```python
import torch

def _softmax(x, axis=-1):
    return torch.softmax(x, dim=axis)
def _gelu(x):
    return torch.nn.functional.gelu(x, approximate="tanh")
def _silu(x):
    return torch.nn.functional.silu(x)
def _sigmoid(x):
    return torch.sigmoid(x)
def _layernorm(x, axis=-1, eps=1e-5):
    m = x.mean(dim=axis, keepdim=True)
    v = x.var(dim=axis, keepdim=True, unbiased=False)
    return (x - m) / torch.sqrt(v + eps)
def _scatter_add(tgt, idx, src, axis=0):
    return tgt.clone().index_add_(axis, idx, src)
def _segment_sum(src, seg, n):
    out = torch.zeros((n,) + src.shape[1:], dtype=src.dtype)
    return out.index_add_(0, seg, src)
def _pad(x, pw):
    flat = tuple(v for pair in reversed(pw) for v in pair)
    return torch.nn.functional.pad(x, flat)

kernel_name = 'interleave_layers__ssm_conv_h3__dense_activation_relu__prompt'

def setup_inputs():

    _w = (lambda a: (lambda t: t.to(torch.bfloat16) if t.is_floating_point() else t)(torch.as_tensor(a)))
    def _A_setup():
        B, S, D = 1, 2048, 512
        ks = 4
        return {
            "q": _w((lambda *s: torch.randn(*s, dtype=torch.bfloat16) * 0.05)(B, S, D)),
            "k": _w((lambda *s: torch.randn(*s, dtype=torch.bfloat16) * 0.8)(B, S, D)),
            "v": _w((lambda *s: torch.randn(*s, dtype=torch.bfloat16) * 0.8)(B, S, D)),
            "shift_k": _w((lambda *s: torch.randn(*s, dtype=torch.bfloat16) * 0.8)(D, ks) * 0.5),
            "ssm_k": _w((lambda *s: torch.randn(*s, dtype=torch.bfloat16) * 0.8)(D, S) * (1.0 / S ** 0.5)),
            "ssm_D": _w((lambda *s: torch.randn(*s, dtype=torch.bfloat16) * 0.8)(D)),
        }
    _A_in = _A_setup()

    def _B_setup():
        B, S, D, Dff = 1, 2048, 512, 1376
        return {
            "x": _w((lambda *s: torch.randn(*s, dtype=torch.bfloat16) * 0.05)(B, S, D)),
            "w1": _w((lambda *s: torch.randn(*s, dtype=torch.bfloat16) * 0.1)(D, Dff)),
            "w2": _w((lambda *s: torch.randn(*s, dtype=torch.bfloat16) * 0.1)(Dff, D)),
        }
    _B_in = _B_setup()

    return {
        "A_q": _A_in["q"],
        "A_k": _A_in["k"],
        "A_v": _A_in["v"],
        "A_shift_k": _A_in["shift_k"],
        "A_ssm_k": _A_in["ssm_k"],
        "A_ssm_D": _A_in["ssm_D"],
        "B_x": _B_in["x"],
        "B_w1": _B_in["w1"],
        "B_w2": _B_in["w2"],
    }

def reference(A_q, A_k, A_v, A_shift_k, A_ssm_k, A_ssm_D, B_x, B_w1, B_w2):
    def _A_ref(q, k, v, shift_k, ssm_k, ssm_D):
        B, S, D = q.shape
        ks = shift_k.shape[-1]
        k_pad = _pad(k, ((0, 0), (ks - 1, 0), (0, 0)))
        k_shift = torch.zeros_like(k)
        for t in range(ks):
            k_shift = k_shift + k_pad[:, t:t + S, :] * shift_k[:, ks - 1 - t][None, None, :]
        kv = k_shift * v
        L = 2 * S
        kv_pad = _pad(kv, ((0, 0), (0, S), (0, 0)))
        kf_pad = _pad(ssm_k, ((0, 0), (0, S)))
        kv_f = (lambda x, axes=(-2,-1): torch.fft.rfft2(x.float(), dim=axes))(kv_pad[:, None, :, :], axes=(1, 2))
        kf_f = (lambda x, axes=(-2,-1): torch.fft.rfft2(x.float(), dim=axes))(kf_pad[:, None, :], axes=(1, 2))
        kf_f = (lambda x, *d: x.permute(*d))(kf_f, 1, 2, 0)
        y_f = kv_f * kf_f[None, :, :, :]
        y = (lambda x, s, axes=(-2,-1): torch.fft.irfft2(x, s=s, dim=axes).to(torch.bfloat16))(y_f, (1, L), axes=(1, 2))[:, 0, :S, :]
        y = y + kv * ssm_D[None, None, :]
        return q * y

    def _B_ref(x, w1, w2):
        h = torch.einsum("bsd,df->bsf", x, w1)
        zero = torch.zeros_like(h)
        h = torch.maximum(h, zero)
        return torch.einsum("bsf,fd->bsd", h, w2)

    x = A_q
    x = x + _A_ref(_layernorm(x, axis=-1), A_k, A_v, A_shift_k, A_ssm_k, A_ssm_D)
    x = x + _B_ref(_layernorm(x, axis=-1), B_w1, B_w2)
    x = x + _A_ref(_layernorm(x, axis=-1), A_k, A_v, A_shift_k, A_ssm_k, A_ssm_D)
    x = x + _B_ref(_layernorm(x, axis=-1), B_w1, B_w2)
    return x
```

```cpp
#include <hip/hip_runtime.h>
#include <hip/hip_bf16.h>
#include <stdint.h>

typedef unsigned short u16;
#define S_ 2048
#define D_ 512
#define F_ 1376

__device__ __forceinline__ float bf2f(u16 v) { return __uint_as_float(((uint32_t)v) << 16); }
__device__ __forceinline__ u16 f2bf(float f) { __hip_bfloat16 b = __float2bfloat16(f); return *(u16*)&b; }
__device__ __forceinline__ float rbf(float f) { return bf2f(f2bf(f)); }

__global__ void k_kv(const u16* k, const u16* v, const u16* sk, u16* kv) {
    int i = blockIdx.x * 256 + threadIdx.x;
    int s = i / D_, d = i % D_;
    float acc = 0.f;
    for (int t = 0; t < 4; ++t) {
        int r = s + t - 3;
        float kk = r >= 0 ? bf2f(k[r * D_ + d]) : 0.f;
        acc = rbf(acc + rbf(kk * bf2f(sk[d * 4 + 3 - t])));
    }
    kv[i] = f2bf(acc * bf2f(v[i]));
}
__global__ void k_conv(const u16* kv, const u16* h, const u16* Dv, u16* y) {
    int i = blockIdx.x * 256 + threadIdx.x;
    int s = i / D_, d = i % D_;
    float acc = 0.f;
    for (int t = 0; t <= s; ++t) acc += bf2f(kv[t * D_ + d]) * bf2f(h[d * S_ + s - t]);
    float yy = rbf(acc);
    y[i] = f2bf(yy + rbf(bf2f(kv[i]) * bf2f(Dv[d])));
}
__device__ void ln_row(float* x, float* o, int lane) {
    float s = 0.f;
    for (int j = 0; j < 8; ++j) s += x[j];
    for (int m = 32; m; m >>= 1) s += __shfl_xor(s, m);
    float mean = s / 512.f;
    float q = 0.f;
    for (int j = 0; j < 8; ++j) q += (x[j] - mean) * (x[j] - mean);
    for (int m = 32; m; m >>= 1) q += __shfl_xor(q, m);
    float mb = rbf(mean), vb = rbf(q / 512.f);
    float den = rbf(sqrtf(rbf(vb + 1e-5f)));
    for (int j = 0; j < 8; ++j) o[j] = rbf(rbf(x[j] - mb) / den);
}
__global__ void k_mix(const u16* x, const u16* y, u16* x1, u16* xn) {
    int row = blockIdx.x * 4 + threadIdx.x / 64, lane = threadIdx.x & 63;
    float a[8], n[8];
    for (int j = 0; j < 8; ++j) a[j] = bf2f(x[row * D_ + lane * 8 + j]);
    ln_row(a, n, lane);
    for (int j = 0; j < 8; ++j) a[j] = rbf(a[j] + rbf(n[j] * bf2f(y[row * D_ + lane * 8 + j])));
    ln_row(a, n, lane);
    for (int j = 0; j < 8; ++j) { x1[row * D_ + lane * 8 + j] = f2bf(a[j]); xn[row * D_ + lane * 8 + j] = f2bf(n[j]); }
}
__global__ void k_g1(const u16* xn, const u16* w1, u16* h) {
    int i = blockIdx.x * 256 + threadIdx.x;
    int s = i / F_, f = i % F_;
    float acc = 0.f;
    for (int k = 0; k < D_; ++k) acc += bf2f(xn[s * D_ + k]) * bf2f(w1[k * F_ + f]);
    h[i] = f2bf(fmaxf(rbf(acc), 0.f));
}
__global__ void k_g2(const u16* h, const u16* w2, const u16* x1, u16* o) {
    int i = blockIdx.x * 256 + threadIdx.x;
    int s = i / D_, d = i % D_;
    float acc = 0.f;
    for (int k = 0; k < F_; ++k) acc += bf2f(h[s * F_ + k]) * bf2f(w2[k * D_ + d]);
    o[i] = f2bf(bf2f(x1[i]) + rbf(acc));
}

extern "C" void kernel_launch(void* const* d_in, const int* in_sizes, int n_in,
                              void* d_out, int out_size, void* d_ws, size_t ws_size,
                              hipStream_t stream) {
    const u16* q = (const u16*)d_in[0]; const u16* k = (const u16*)d_in[1]; const u16* v = (const u16*)d_in[2];
    const u16* sk = (const u16*)d_in[3]; const u16* ssmk = (const u16*)d_in[4]; const u16* Dv = (const u16*)d_in[5];
    const u16* w1 = (const u16*)d_in[7]; const u16* w2 = (const u16*)d_in[8];
    u16* ws = (u16*)d_ws;
    const int N = S_ * D_;
    u16* kv = ws; u16* y = ws + N; u16* x1 = ws + 2 * N; u16* xn = ws + 3 * N; u16* x2 = ws + 4 * N; u16* h = ws + 5 * N;
    k_kv<<<N / 256, 256, 0, stream>>>(k, v, sk, kv);
    k_conv<<<N / 256, 256, 0, stream>>>(kv, ssmk, Dv, y);
    k_mix<<<S_ / 4, 256, 0, stream>>>(q, y, x1, xn);
    k_g1<<<S_ * F_ / 256, 256, 0, stream>>>(xn, w1, h);
    k_g2<<<N / 256, 256, 0, stream>>>(h, w2, x1, x2);
    k_mix<<<S_ / 4, 256, 0, stream>>>(x2, y, x1, xn);
    k_g1<<<S_ * F_ / 256, 256, 0, stream>>>(xn, w1, h);
    k_g2<<<N / 256, 256, 0, stream>>>(h, w2, x1, (u16*)d_out);
}
```

```cpp
#include <hip/hip_runtime.h>
#include <hip/hip_bf16.h>
#include <stdint.h>

typedef unsigned short u16;
typedef unsigned int u32;
typedef u32 v4u __attribute__((ext_vector_type(4)));
typedef u32 v2u __attribute__((ext_vector_type(2)));
typedef float f32x4 __attribute__((ext_vector_type(4)));
typedef float f32x2 __attribute__((ext_vector_type(2)));
typedef short bf16x8 __attribute__((ext_vector_type(8)));
typedef short s16x4 __attribute__((ext_vector_type(4)));
typedef short s16x2 __attribute__((ext_vector_type(2)));
typedef __bf16 bf16x2 __attribute__((ext_vector_type(2)));
#define LAS __attribute__((address_space(3)))
#define GAS __attribute__((address_space(1)))
#define DEV __device__ __forceinline__

constexpr int S_ = 2048, D_ = 512, F_ = 1376;
constexpr int NP = 43;
constexpr int NT = 86;
constexpr size_t MB = 1u << 20;
constexpr size_t OFF_UR = 0, OFF_X0T = 2 * MB, OFF_Y = 4 * MB, OFF_X1 = 6 * MB, OFF_X2 = 8 * MB, OFF_X3 = 10 * MB,
                 OFF_H = 12 * MB, OFF_W1T = 18 * MB, OFF_W2T = 20 * MB, OFF_ST0 = 22 * MB, OFF_ST2 = 22 * MB + 65536,
                 OFF_CS = 22 * MB + 262144;

DEV float lo2f(u32 d) { return __builtin_bit_cast(float, d << 16); }
DEV float hi2f(u32 d) { return __builtin_bit_cast(float, d & 0xffff0000u); }
DEV u32 pk2bf(float a, float b) {
    f32x2 x = {a, b};
    return __builtin_bit_cast(u32, __builtin_convertvector(x, bf16x2));
}
DEV f32x4 mfma(v4u a, v4u b, f32x4 c) {
    return __builtin_amdgcn_mfma_f32_16x16x32_bf16(__builtin_bit_cast(bf16x8, a), __builtin_bit_cast(bf16x8, b), c, 0, 0, 0);
}
DEV v2u tr_read(const LAS char* p) {
    return __builtin_bit_cast(v2u, __builtin_amdgcn_ds_read_tr16_b64_v4i16((LAS s16x4*)p));
}
DEV float dot2(u32 a, u32 b, float c) {
    return __builtin_amdgcn_fdot2_f32_bf16(__builtin_bit_cast(bf16x2, a), __builtin_bit_cast(bf16x2, b), c, false);
}
DEV float xadd16(float v) {
    auto r = __builtin_amdgcn_permlane16_swap(__builtin_bit_cast(u32, v), __builtin_bit_cast(u32, v), false, false);
    const u32 r0 = r[0], r1 = r[1];
    return __builtin_bit_cast(float, r0) + __builtin_bit_cast(float, r1);
}
DEV float xadd32(float v) {
    auto r = __builtin_amdgcn_permlane32_swap(__builtin_bit_cast(u32, v), __builtin_bit_cast(u32, v), false, false);
    const u32 r0 = r[0], r1 = r[1];
    return __builtin_bit_cast(float, r0) + __builtin_bit_cast(float, r1);
}
template <int CTRL> DEV float dppadd(float v) {
    return v + __builtin_bit_cast(float, __builtin_amdgcn_update_dpp(0, __builtin_bit_cast(int, v), CTRL, 0xf, 0xf, true));
}
DEV float row16sum(float v) {
    v = dppadd<0x121>(v);
    v = dppadd<0x122>(v);
    v = dppadd<0x124>(v);
    v = dppadd<0x128>(v);
    return v;
}
#define SB() __builtin_amdgcn_sched_barrier(0)

__global__ __launch_bounds__(512) void k_prep(const u16* __restrict__ q, const u16* __restrict__ k, const u16* __restrict__ v,
                                              const u16* __restrict__ sk, const u16* __restrict__ w1,
                                              const u16* __restrict__ w2, char* __restrict__ ws) {
    __shared__ __attribute__((aligned(16))) char smem[16384];
    const int b = blockIdx.x, tid = threadIdx.x, lane = tid & 63, w = __builtin_amdgcn_readfirstlane(tid >> 6);
    const int c = lane & 15, g = lane >> 4;
    if (b < 256) {
        const int cb = b & 7, tb = b >> 3;
        const int row = tid >> 3, c8 = tid & 7, s = 64 * tb + row, ch0 = 64 * cb + 8 * c8;
        v4u kk[4];
#pragma unroll
        for (int t = 0; t < 4; ++t) {
            const int r = s + t - 3;
            v4u z = {0, 0, 0, 0};
            kk[t] = r >= 0 ? *(const v4u*)(k + (size_t)r * D_ + ch0) : z;
        }
        const v4u vv = *(const v4u*)(v + (size_t)s * D_ + ch0);
        const v4u qq = *(const v4u*)(q + (size_t)s * D_ + ch0);
        v4u sw[4];
#pragma unroll
        for (int t = 0; t < 4; ++t) sw[t] = *(const v4u*)(sk + ch0 * 4 + 8 * t);
        const int srow = 8 * b + w;
        const v4u xr = *(const v4u*)(q + (size_t)srow * D_ + lane * 8);
        *(LAS v4u*)((LAS char*)smem + 8192 + row * 128 + c8 * 16) = qq;
        v4u o;
#pragma unroll
        for (int j = 0; j < 4; ++j) {
            float a = lo2f(kk[0][j]) * hi2f(sw[j].y);
            a = fmaf(lo2f(kk[1][j]), lo2f(sw[j].y), a);
            a = fmaf(lo2f(kk[2][j]), hi2f(sw[j].x), a);
            a = fmaf(lo2f(kk[3][j]), lo2f(sw[j].x), a);
            float bb = hi2f(kk[0][j]) * hi2f(sw[j].w);
            bb = fmaf(hi2f(kk[1][j]), lo2f(sw[j].w), bb);
            bb = fmaf(hi2f(kk[2][j]), hi2f(sw[j].z), bb);
            bb = fmaf(hi2f(kk[3][j]), lo2f(sw[j].z), bb);
            o[j] = pk2bf(a * lo2f(vv[j]), bb * hi2f(vv[j]));
        }
        *(LAS v4u*)((LAS char*)smem + row * 128 + c8 * 16) = o;
        float x[8];
#pragma unroll
        for (int j = 0; j < 4; ++j) { x[2 * j] = lo2f(xr[j]); x[2 * j + 1] = hi2f(xr[j]); }
        float sm = ((x[0] + x[1]) + (x[2] + x[3])) + ((x[4] + x[5]) + (x[6] + x[7]));
        sm = xadd32(xadd16(row16sum(sm)));
        const float mean = sm * (1.f / 512.f);
        float sq = 0.f;
#pragma unroll
        for (int j = 0; j < 8; ++j) sq = fmaf(x[j] - mean, x[j] - mean, sq);
        sq = xadd32(xadd16(row16sum(sq)));
        const float rs = __builtin_amdgcn_rsqf(sq * (1.f / 512.f) + 1e-5f);
        if (lane == 0) *(f32x2*)(ws + OFF_ST0 + srow * 8) = f32x2{rs, -mean * rs};
        __syncthreads();
        const int arr = w >> 2, cw = w & 3, qd = c >> 2, p = c & 3;
        u16* dst = (u16*)(ws + (arr ? OFF_X0T : OFF_UR)) + (size_t)(64 * cb + 16 * cw + c) * S_ + (arr ? 64 * tb : 1984 - 64 * tb);
#pragma unroll
        for (int i = 0; i < 2; ++i) {
            const int oc = 4 * i + g;
            const int r0 = arr ? 8 * oc + qd : 63 - 8 * oc - qd, r1 = arr ? r0 + 4 : r0 - 4;
            const LAS char* base = (const LAS char*)smem + arr * 8192 + 32 * cw + 8 * p;
            const v2u a = tr_read(base + r0 * 128), bb = tr_read(base + r1 * 128);
            *(v4u*)(dst + 8 * oc) = v4u{a.x, a.y, bb.x, bb.y};
        }
    } else if (b < 256 + NP) {
        const int p = b - 256;
        u32 d[2][8];
#pragma unroll
        for (int u = 0; u < 2; ++u)
#pragma unroll
            for (int e = 0; e < 8; ++e) {
                const int kr = 32 * (2 * w + u) + 16 * (e >> 2) + 4 * g + (e & 3);
                d[u][e] = *(const u32*)(w1 + (size_t)kr * F_ + 32 * p + 2 * c);
            }
        float s0 = 0.f, s1 = 0.f;
#pragma unroll
        for (int u = 0; u < 2; ++u) {
            v4u fa, fb;
#pragma unroll
            for (int i = 0; i < 4; ++i) {
                fa[i] = __builtin_amdgcn_perm(d[u][2 * i + 1], d[u][2 * i], 0x05040100u);
                fb[i] = __builtin_amdgcn_perm(d[u][2 * i + 1], d[u][2 * i], 0x07060302u);
            }
#pragma unroll
            for (int e = 0; e < 8; ++e) { s0 += lo2f(d[u][e]); s1 += hi2f(d[u][e]); }
            char* dst = ws + OFF_W1T + ((size_t)(2 * p) * 16 + 2 * w + u) * 1024 + lane * 16;
            *(v4u*)dst = fa;
            *(v4u*)(dst + 16384) = fb;
        }
        s0 = xadd32(xadd16(s0));
        s1 = xadd32(xadd16(s1));
        LAS float* red = (LAS float*)smem;
        if (g == 0) { red[w * 32 + c] = s0; red[w * 32 + 16 + c] = s1; }
        __syncthreads();
        if (tid < 32) {
            float t = 0.f;
#pragma unroll
            for (int i = 0; i < 8; ++i) t += red[i * 32 + tid];
            ((float*)(ws + OFF_CS))[32 * p + tid] = t;
        }
    } else {
        const int p = b - 256 - NP;
        v2u d[8];
#pragma unroll
        for (int e = 0; e < 8; ++e) {
            const int f = 32 * p + 2 * (4 * g + (e & 3)) + (e >> 2);
            d[e] = *(const v2u*)(w2 + (size_t)f * D_ + 64 * w + 4 * c);
        }
        char* dst = ws + OFF_W2T + ((size_t)(w * NP + p) * 4) * 1024 + lane * 16;
#pragma unroll
        for (int ct = 0; ct < 4; ++ct) {
            v4u f;
#pragma unroll
            for (int i = 0; i < 4; ++i)
                f[i] = __builtin_amdgcn_perm(d[2 * i + 1][ct >> 1], d[2 * i][ct >> 1], (ct & 1) ? 0x07060302u : 0x05040100u);
            *(v4u*)(dst + ct * 1024) = f;
        }
    }
}

constexpr int CV_E = 0, CV_O = 4160, CV_U = 8320, CV_CH = 12928, CV_X = 2 * CV_CH, CV_BYTES = CV_X + 16384;

__global__ __launch_bounds__(256) void k_conv(const u16* __restrict__ ssmk, const u16* __restrict__ Dv, char* __restrict__ ws) {
    __shared__ __attribute__((aligned(16))) char smem_[CV_BYTES];
    LAS char* smem = (LAS char*)smem_;
    const int b = blockIdx.x, tid = threadIdx.x, lane = tid & 63, w = __builtin_amdgcn_readfirstlane(tid >> 6);
    const int c = lane & 15, g = lane >> 4;
    const int ch0 = 64 * (b & 7) + 2 * (b >> 3);
    const int cw = w >> 1, half = w & 1, ch = ch0 + cw;
    v4u hh[2], uu[2];
    u32 hn[2];
#pragma unroll
    for (int i = 0; i < 2; ++i) {
        uu[i] = *(const v4u*)(ws + OFF_UR + (size_t)(ch0 + i) * 4096 + tid * 16);
        hh[i] = *(const v4u*)(ssmk + (size_t)(ch0 + i) * S_ + tid * 8);
        hn[i] = tid < 255 ? *(const u32*)(ssmk + (size_t)(ch0 + i) * S_ + tid * 8 + 8) : 0u;
    }
    v2u x0[4];
    f32x4 sa[4], sb[4];
#pragma unroll
    for (int i = 0; i < 4; ++i) {
        const int s0 = 256 * (4 * half + i) + 16 * c + 4 * g;
        x0[i] = *(const v2u*)(ws + OFF_X0T + (size_t)ch * 4096 + s0 * 2);
        sa[i] = *(const f32x4*)(ws + OFF_ST0 + s0 * 8);
        sb[i] = *(const f32x4*)(ws + OFF_ST0 + s0 * 8 + 16);
    }
    const float dval = lo2f(Dv[ch]);
    SB();
    const v4u zero = {0, 0, 0, 0};
    if (tid < 60) {
        const int i = tid >= 30, r = tid - 30 * i;
        *(LAS v4u*)(smem + i * CV_CH + CV_U + 4096 + r * 16) = zero;
    } else if (tid >= 64 && tid < 70) {
        const int r = tid - 64, i = r >= 3, m = r - 3 * i;
        *(LAS v4u*)(smem + i * CV_CH + (m < 2 ? CV_E + 16 * m : CV_O)) = zero;
    }
#pragma unroll
    for (int i = 0; i < 2; ++i) {
        *(LAS v4u*)(smem + i * CV_CH + CV_U + tid * 16) = uu[i];
        *(LAS v4u*)(smem + i * CV_CH + CV_E + 32 + tid * 16) = hh[i];
        v4u o;
        o.x = __builtin_amdgcn_alignbyte(hh[i].y, hh[i].x, 2);
        o.y = __builtin_amdgcn_alignbyte(hh[i].z, hh[i].y, 2);
        o.z = __builtin_amdgcn_alignbyte(hh[i].w, hh[i].z, 2);
        o.w = __builtin_amdgcn_alignbyte(hn[i], hh[i].w, 2);
        *(LAS v4u*)(smem + i * CV_CH + CV_O + 32 + tid * 16) = o;
        if (tid == 0) *(LAS v4u*)(smem + i * CV_CH + CV_O + 16) = v4u{0, 0, 0, hh[i].x << 16};
    }
    __syncthreads();
    f32x4 acc[8];
#pragma unroll
    for (int i = 0; i < 8; ++i) acc[i] = f32x4{0.f, 0.f, 0.f, 0.f};
    const LAS char* ab = smem + cw * CV_CH + ((c & 1) ? CV_E + 2 * (1 + c) : CV_O + 2 * c) + 16 * g + 256 * half;
    const LAS char* bb = smem + cw * CV_CH + CV_U + 4064 - 3584 - 32 * c + 16 * g + 256 * half;
#pragma unroll
    for (int r = 0; r < 4; ++r) {
        v4u A[8], B[8];
#pragma unroll
        for (int i = 0; i < 8; ++i) {
            const LAS u32* pa = (const LAS u32*)(ab + 512 * i + 64 * r);
            A[i] = v4u{pa[0], pa[1], pa[2], pa[3]};
            B[i] = *(const LAS v4u*)(bb + 512 * (7 - i) + 64 * r);
        }
#pragma unroll
        for (int al = 0; al < 8; ++al)
#pragma unroll
            for (int be = 0; be + al < 8; ++be) acc[al + be] = mfma(A[al], B[be], acc[al + be]);
    }
    LAS f32x4* xch = (LAS f32x4*)(smem + CV_X);
    f32x4 fin[4];
    if (half) {
#pragma unroll
        for (int i = 0; i < 4; ++i) { xch[(w * 4 + i) * 64 + lane] = acc[i]; fin[i] = acc[4 + i]; }
    } else {
#pragma unroll
        for (int i = 0; i < 4; ++i) { xch[(w * 4 + i) * 64 + lane] = acc[4 + i]; fin[i] = acc[i]; }
    }
    __syncthreads();
#pragma unroll
    for (int i = 0; i < 4; ++i) {
        const int I = 4 * half + i, s0 = 256 * I + 16 * c + 4 * g;
        const f32x4 y = fin[i] + xch[((w ^ 1) * 4 + i) * 64 + lane];
        const v2u u = *(const LAS v2u*)(smem + cw * CV_CH + CV_U + 2 * (2044 - s0));
        const float y0 = fmaf(hi2f(u.y), dval, y[0]), y1 = fmaf(lo2f(u.y), dval, y[1]);
        const float y2 = fmaf(hi2f(u.x), dval, y[2]), y3 = fmaf(lo2f(u.x), dval, y[3]);
        const float a0 = lo2f(x0[i].x), a1 = hi2f(x0[i].x), a2 = lo2f(x0[i].y), a3 = hi2f(x0[i].y);
        const float z0 = fmaf(fmaf(a0, sa[i][0], sa[i][1]), y0, a0), z1 = fmaf(fmaf(a1, sa[i][2], sa[i][3]), y1, a1);
        const float z2 = fmaf(fmaf(a2, sb[i][0], sb[i][1]), y2, a2), z3 = fmaf(fmaf(a3, sb[i][2], sb[i][3]), y3, a3);
        const size_t off = (size_t)(4 * I + (c >> 2)) * 65536 + ch * 128 + (((c & 3) ^ ((ch >> 1) & 3)) << 5) + 8 * g;
        *(v2u*)(ws + OFF_Y + off) = v2u{pk2bf(y0, y1), pk2bf(y2, y3)};
        *(v2u*)(ws + OFF_X1 + off) = v2u{pk2bf(z0, z1), pk2bf(z2, z3)};
    }
}

template <bool MIX>
__global__ __launch_bounds__(256) void k_g1(char* __restrict__ ws) {
    __shared__ __attribute__((aligned(16))) char smem_[65536 + 2048 + 512];
    LAS char* smem = (LAS char*)smem_;
    const int b = blockIdx.x, tid = threadIdx.x, lane = tid & 63, w = __builtin_amdgcn_readfirstlane(tid >> 6);
    const int c = lane & 15, g = lane >> 4;
    const int tb = 4 * (b & 7) + (b >> 6), cg = (b >> 3) & 7;
    const char* xsrc = ws + (MIX ? OFF_X2 : OFF_X1) + (size_t)tb * 65536 + w * 16384 + lane * 16;
    const char* ysrc = ws + OFF_Y + (size_t)tb * 65536 + w * 16384 + lane * 16;
    f32x2 pst[8];
    if (MIX) {
#pragma unroll
        for (int i = 0; i < 8; ++i) pst[i] = *(const f32x2*)(ws + OFF_ST2 + (tb * 8 + i) * 512 + lane * 8);
    }
    v4u xs[16], ys[16];
#pragma unroll
    for (int i = 0; i < 16; ++i) {
        xs[i] = *(const v4u*)(xsrc + i * 1024);
        if (MIX) ys[i] = *(const v4u*)(ysrc + i * 1024);
    }
    const int T0 = 11 * cg + 3 * w, Tend = cg == 7 ? NT : 11 * cg + 11;
    const __amdgpu_buffer_rsrc_t rw = __builtin_amdgcn_make_buffer_rsrc(ws + OFF_W1T, 0, NT * 16384, 0x00020000);
    int vo[3];
#pragma unroll
    for (int j = 0; j < 3; ++j) vo[j] = T0 + j < Tend ? (T0 + j) * 16384 + lane * 16 : 0x40000000;
    v4u W[16][3];
    constexpr int KE = MIX ? 7 : 14;
#pragma unroll
    for (int kk = 0; kk < KE; ++kk)
#pragma unroll
        for (int j = 0; j < 3; ++j) W[kk][j] = __builtin_amdgcn_raw_buffer_load_b128(rw, vo[j], kk * 1024, 0);
    SB();
    if (MIX) {
        float sm = 0.f, sq = 0.f;
#pragma unroll
        for (int i = 0; i < 8; ++i) { sm += pst[i].x; sq += pst[i].y; }
        const float mean = sm * (1.f / 512.f);
        const float rs = __builtin_amdgcn_rsqf(fmaxf(sq * (1.f / 512.f) - mean * mean, 0.f) + 1e-5f);
        LAS f32x2* st = (LAS f32x2*)(smem + 65536 + w * 512);
        st[lane] = f32x2{rs, -mean * rs};
        const int tok0 = 16 * (((lane & 7) >> 1) ^ g) + 8 * (lane & 1);
        f32x4 ab[4];
#pragma unroll
        for (int i = 0; i < 4; ++i) ab[i] = *(LAS f32x4*)(st + tok0 + 2 * i);
        const bool keep = (cg >> 1) == w;
        char* x3 = ws + OFF_X3 + (size_t)tb * 65536 + w * 16384 + lane * 16;
#pragma unroll
        for (int i = 0; i < 16; ++i) {
            v4u o;
#pragma unroll
            for (int j = 0; j < 4; ++j) {
                const float x0 = lo2f(xs[i][j]), x1 = hi2f(xs[i][j]);
                o[j] = pk2bf(fmaf(fmaf(x0, ab[j][0], ab[j][1]), lo2f(ys[i][j]), x0),
                             fmaf(fmaf(x1, ab[j][2], ab[j][3]), hi2f(ys[i][j]), x1));
            }
            *(LAS v4u*)(smem + w * 16384 + i * 1024 + lane * 16) = o;
            if (keep && (i >> 3) == (cg & 1)) *(v4u*)(x3 + i * 1024) = o;
            if (i < 9) {
#pragma unroll
                for (int j = 0; j < 3; ++j) W[KE + i][j] = __builtin_amdgcn_raw_buffer_load_b128(rw, vo[j], (KE + i) * 1024, 0);
            }
            SB();
        }
    } else {
#pragma unroll
        for (int i = 0; i < 16; ++i) {
            *(LAS v4u*)(smem + w * 16384 + i * 1024 + lane * 16) = xs[i];
            if (i >= 3 && i < 5) {
#pragma unroll
                for (int j = 0; j < 3; ++j) W[KE + i - 3][j] = __builtin_amdgcn_raw_buffer_load_b128(rw, vo[j], (KE + i - 3) * 1024, 0);
            }
            SB();
        }
    }
    f32x4 cs[3];
#pragma unroll
    for (int j = 0; j < 3; ++j) cs[j] = *(const f32x4*)(ws + OFF_CS + (T0 + j) * 64 + g * 16);
    __syncthreads();
    const LAS char* xb[4];
#pragma unroll
    for (int i = 0; i < 4; ++i)
        xb[i] = smem + (4 * g + (c >> 2)) * 128 + ((((i + w) & 3) ^ ((2 * g + (c >> 3)) & 3)) << 5) + 8 * (c & 3);
    f32x4 acc[3][4];
#pragma unroll
    for (int j = 0; j < 3; ++j)
#pragma unroll
        for (int i = 0; i < 4; ++i) acc[j][i] = f32x4{0.f, 0.f, 0.f, 0.f};
    float sm = 0.f, sq = 0.f;
#pragma unroll
    for (int kk = 0; kk < 16; ++kk) {
        v4u X[4];
#pragma unroll
        for (int i = 0; i < 4; ++i) {
            const v2u lo = tr_read(xb[i] + kk * 4096), hi = tr_read(xb[i] + kk * 4096 + 2048);
            X[i] = v4u{lo.x, lo.y, hi.x, hi.y};
        }
#pragma unroll
        for (int j = 0; j < 3; ++j)
#pragma unroll
            for (int i = 0; i < 4; ++i) acc[j][i] = mfma(W[kk][j], X[i], acc[j][i]);
#pragma unroll
        for (int e = 0; e < 4; ++e) { sm = dot2(X[0][e], 0x3f803f80u, sm); sq = dot2(X[0][e], X[0][e], sq); }
    }
    sm = xadd32(xadd16(sm));
    sq = xadd32(xadd16(sq));
    {
        const float mean = sm * (1.f / 512.f);
        const float rs = __builtin_amdgcn_rsqf(fmaxf(sq * (1.f / 512.f) - mean * mean, 0.f) + 1e-5f);
        if (g == 0) *(LAS f32x2*)(smem + 65536 + 2048 + (w * 16 + c) * 8) = f32x2{rs, -mean * rs};
    }
    __syncthreads();
#pragma unroll
    for (int i = 0; i < 4; ++i) {
        const int tt = (i + w) & 3;
        const f32x2 ab = *(LAS f32x2*)(smem + 65536 + 2048 + (tt * 16 + c) * 8);
#pragma unroll
        for (int j = 0; j < 3; ++j) {
            const int T = T0 + j;
            const f32x4 u = acc[j][i] * ab.x + cs[j] * ab.y;
            const s16x2 z = {0, 0};
            const v2u o = {__builtin_bit_cast(u32, __builtin_elementwise_max(__builtin_bit_cast(s16x2, pk2bf(u[0], u[1])), z)),
                           __builtin_bit_cast(u32, __builtin_elementwise_max(__builtin_bit_cast(s16x2, pk2bf(u[2], u[3])), z))};
            if (T < Tend) *(v2u*)(ws + OFF_H + ((size_t)(tb * NP + (T >> 1)) * 4 + tt) * 1024 + lane * 16 + 8 * (T & 1)) = o;
        }
    }
}

template <bool FINAL>
__global__ __launch_bounds__(256) void k_g2(char* __restrict__ ws, u16* __restrict__ out) {
    __shared__ __attribute__((aligned(16))) char smem_[49152];
    LAS char* smem = (LAS char*)smem_;
    const int b = blockIdx.x, tid = threadIdx.x, lane = tid & 63, w = __builtin_amdgcn_readfirstlane(tid >> 6);
    const int c = lane & 15, g = lane >> 4;
    const int tb = 4 * (b & 7) + (b >> 6), cg = (b >> 3) & 7;
    const __amdgpu_buffer_rsrc_t rh = __builtin_amdgcn_make_buffer_rsrc(ws + OFF_H + (size_t)tb * NP * 4096, 0, NP * 4096, 0x00020000);
    const __amdgpu_buffer_rsrc_t rw = __builtin_amdgcn_make_buffer_rsrc(ws + OFF_W2T + (size_t)cg * NP * 4096, 0, NP * 4096, 0x00020000);
    int ho[4];
#pragma unroll
    for (int i = 0; i < 4; ++i) ho[i] = w * 4096 + ((i + w) & 3) * 1024 + lane * 16;
    const int wo = w * 4096 + lane * 16;
    const int inv = w == 3 ? 0x40000000 : 0;
    v4u H[11][4], W[11][4];
    constexpr int PRE = 7;
#define G2_LOAD(s)                                                                                              \
    {                                                                                                           \
        _Pragma("unroll") for (int i = 0; i < 4; ++i) {                                                         \
            H[s][i] = __builtin_amdgcn_raw_buffer_load_b128(rh, ho[i] + ((s) == 10 ? inv : 0), (s) * 16384, 0); \
            W[s][i] = __builtin_amdgcn_raw_buffer_load_b128(rw, wo + i * 1024 + ((s) == 10 ? inv : 0), (s) * 16384, 0); \
        }                                                                                                       \
    }
#pragma unroll
    for (int s = 0; s < PRE; ++s) G2_LOAD(s)
    v2u res[4];
#pragma unroll
    for (int ct = 0; ct < 4; ++ct) {
        const int ch = 64 * cg + 4 * c + ct;
        res[ct] = *(const v2u*)(ws + (FINAL ? OFF_X3 : OFF_X1) + (size_t)tb * 65536 + ch * 128 + ((w ^ ((ch >> 1) & 3)) << 5) + 8 * g);
    }
    SB();
    f32x4 acc[4][4];
#pragma unroll
    for (int i = 0; i < 4; ++i)
#pragma unroll
        for (int ct = 0; ct < 4; ++ct) acc[i][ct] = f32x4{0.f, 0.f, 0.f, 0.f};
#pragma unroll
    for (int s = 0; s < 11; ++s) {
#pragma unroll
        for (int i = 0; i < 4; ++i)
#pragma unroll
            for (int ct = 0; ct < 4; ++ct) acc[i][ct] = mfma(H[s][i], W[s][ct], acc[i][ct]);
        SB();
        if (s + PRE < 11) G2_LOAD(s + PRE)
        SB();
    }
    LAS f32x4* red = (LAS f32x4*)smem;
#pragma unroll
    for (int i = 1; i < 4; ++i)
#pragma unroll
        for (int ct = 0; ct < 4; ++ct) red[((((i + w) & 3) * 3 + (i - 1)) * 4 + ct) * 64 + lane] = acc[i][ct];
    __syncthreads();
    u32 xo[4][2];
    float xf[4][4];
#pragma unroll
    for (int ct = 0; ct < 4; ++ct) {
        const f32x4 t = (acc[0][ct] + red[((w * 3 + 0) * 4 + ct) * 64 + lane]) +
                        (red[((w * 3 + 1) * 4 + ct) * 64 + lane] + red[((w * 3 + 2) * 4 + ct) * 64 + lane]);
        xf[ct][0] = lo2f(res[ct].x) + t[0];
        xf[ct][1] = hi2f(res[ct].x) + t[1];
        xf[ct][2] = lo2f(res[ct].y) + t[2];
        xf[ct][3] = hi2f(res[ct].y) + t[3];
    }
    if (FINAL) {
#pragma unroll
        for (int r = 0; r < 4; ++r)
            *(v2u*)(out + (size_t)(64 * tb + 16 * w + 4 * g + r) * D_ + 64 * cg + 4 * c) =
                v2u{pk2bf(xf[0][r], xf[1][r]), pk2bf(xf[2][r], xf[3][r])};
    } else {
        float sm[4] = {0.f, 0.f, 0.f, 0.f}, sq[4] = {0.f, 0.f, 0.f, 0.f};
#pragma unroll
        for (int ct = 0; ct < 4; ++ct) {
            const int ch = 64 * cg + 4 * c + ct;
            xo[ct][0] = pk2bf(xf[ct][0], xf[ct][1]);
            xo[ct][1] = pk2bf(xf[ct][2], xf[ct][3]);
            *(v2u*)(ws + OFF_X2 + (size_t)tb * 65536 + ch * 128 + ((w ^ ((ch >> 1) & 3)) << 5) + 8 * g) = v2u{xo[ct][0], xo[ct][1]};
            const float x0 = lo2f(xo[ct][0]), x1 = hi2f(xo[ct][0]), x2 = lo2f(xo[ct][1]), x3 = hi2f(xo[ct][1]);
            sm[0] += x0; sm[1] += x1; sm[2] += x2; sm[3] += x3;
            sq[0] = fmaf(x0, x0, sq[0]); sq[1] = fmaf(x1, x1, sq[1]); sq[2] = fmaf(x2, x2, sq[2]); sq[3] = fmaf(x3, x3, sq[3]);
        }
#pragma unroll
        for (int r = 0; r < 4; ++r) { sm[r] = row16sum(sm[r]); sq[r] = row16sum(sq[r]); }
        if (c == 0) {
            char* st = ws + OFF_ST2 + (tb * 8 + cg) * 512 + (16 * w + 4 * g) * 8;
            *(f32x4*)st = f32x4{sm[0], sq[0], sm[1], sq[1]};
            *(f32x4*)(st + 16) = f32x4{sm[2], sq[2], sm[3], sq[3]};
        }
    }
}

extern "C" void kernel_launch(void* const* d_in, const int* in_sizes, int n_in, void* d_out, int out_size, void* d_ws,
                              size_t ws_size, hipStream_t stream) {
    const u16* q = (const u16*)d_in[0];
    const u16* k = (const u16*)d_in[1];
    const u16* v = (const u16*)d_in[2];
    const u16* sk = (const u16*)d_in[3];
    const u16* ssmk = (const u16*)d_in[4];
    const u16* Dv = (const u16*)d_in[5];
    const u16* w1 = (const u16*)d_in[7];
    const u16* w2 = (const u16*)d_in[8];
    char* ws = (char*)d_ws;
    k_prep<<<256 + 2 * NP, 512, 0, stream>>>(q, k, v, sk, w1, w2, ws);
    k_conv<<<256, 256, 0, stream>>>(ssmk, Dv, ws);
    k_g1<false><<<256, 256, 0, stream>>>(ws);
    k_g2<false><<<256, 256, 0, stream>>>(ws, (u16*)d_out);
    k_g1<true><<<256, 256, 0, stream>>>(ws);
    k_g2<true><<<256, 256, 0, stream>>>(ws, (u16*)d_out);
}
```

```cpp
#include <hip/hip_runtime.h>
#include <hip/hip_bf16.h>
#include <stdint.h>

typedef unsigned short u16;
typedef unsigned int u32;
typedef u32 v4u __attribute__((ext_vector_type(4)));
typedef u32 v2u __attribute__((ext_vector_type(2)));
typedef float f32x4 __attribute__((ext_vector_type(4)));
typedef float f32x2 __attribute__((ext_vector_type(2)));
typedef short bf16x8 __attribute__((ext_vector_type(8)));
typedef short s16x4 __attribute__((ext_vector_type(4)));
typedef short s16x2 __attribute__((ext_vector_type(2)));
typedef __bf16 bf16x2 __attribute__((ext_vector_type(2)));
#define LAS __attribute__((address_space(3)))
#define GAS __attribute__((address_space(1)))
#define DEV __device__ __forceinline__

constexpr int S_ = 2048, D_ = 512, F_ = 1376;
constexpr int NP = 43;
constexpr int NT = 86;
constexpr size_t MB = 1u << 20;
constexpr size_t OFF_UR = 0, OFF_X0T = 2 * MB, OFF_Y = 4 * MB, OFF_X1 = 6 * MB, OFF_X2 = 8 * MB, OFF_X3 = 10 * MB,
                 OFF_H = 12 * MB, OFF_W1T = 18 * MB, OFF_W2T = 20 * MB, OFF_ST0 = 22 * MB, OFF_ST2 = 22 * MB + 65536,
                 OFF_CS = 22 * MB + 262144;

DEV float lo2f(u32 d) { return __builtin_bit_cast(float, d << 16); }
DEV float hi2f(u32 d) { return __builtin_bit_cast(float, d & 0xffff0000u); }
DEV u32 pk2bf(float a, float b) {
    f32x2 x = {a, b};
    return __builtin_bit_cast(u32, __builtin_convertvector(x, bf16x2));
}
DEV f32x4 mfma(v4u a, v4u b, f32x4 c) {
    return __builtin_amdgcn_mfma_f32_16x16x32_bf16(__builtin_bit_cast(bf16x8, a), __builtin_bit_cast(bf16x8, b), c, 0, 0, 0);
}
DEV v2u tr_read(const LAS char* p) {
    return __builtin_bit_cast(v2u, __builtin_amdgcn_ds_read_tr16_b64_v4i16((LAS s16x4*)p));
}
DEV float dot2(u32 a, u32 b, float c) {
    return __builtin_amdgcn_fdot2_f32_bf16(__builtin_bit_cast(bf16x2, a), __builtin_bit_cast(bf16x2, b), c, false);
}
DEV float xadd16(float v) {
    auto r = __builtin_amdgcn_permlane16_swap(__builtin_bit_cast(u32, v), __builtin_bit_cast(u32, v), false, false);
    const u32 r0 = r[0], r1 = r[1];
    return __builtin_bit_cast(float, r0) + __builtin_bit_cast(float, r1);
}
DEV float xadd32(float v) {
    auto r = __builtin_amdgcn_permlane32_swap(__builtin_bit_cast(u32, v), __builtin_bit_cast(u32, v), false, false);
    const u32 r0 = r[0], r1 = r[1];
    return __builtin_bit_cast(float, r0) + __builtin_bit_cast(float, r1);
}
template <int CTRL> DEV float dppadd(float v) {
    return v + __builtin_bit_cast(float, __builtin_amdgcn_update_dpp(0, __builtin_bit_cast(int, v), CTRL, 0xf, 0xf, true));
}
DEV float row16sum(float v) {
    v = dppadd<0x121>(v);
    v = dppadd<0x122>(v);
    v = dppadd<0x124>(v);
    v = dppadd<0x128>(v);
    return v;
}
#define SB() __builtin_amdgcn_sched_barrier(0)
#define STAMP(kid, i)
#define STAMPW(kid, i)

__global__ __launch_bounds__(512) void k_prep(const u16* __restrict__ q, const u16* __restrict__ k, const u16* __restrict__ v,
                                              const u16* __restrict__ sk, const u16* __restrict__ w1,
                                              const u16* __restrict__ w2, char* __restrict__ ws) {
    __shared__ __attribute__((aligned(16))) char smem[16384];
    const int b = blockIdx.x, tid = threadIdx.x, lane = tid & 63, w = __builtin_amdgcn_readfirstlane(tid >> 6);
    const int c = lane & 15, g = lane >> 4;
    STAMP(0, 0);
    if (b < 256) {
        const int cb = b & 7, tb = b >> 3;
        const int row = tid >> 3, c8 = tid & 7, s = 64 * tb + row, ch0 = 64 * cb + 8 * c8;
        v4u kk[4];
#pragma unroll
        for (int t = 0; t < 4; ++t) {
            const int r = s + t - 3;
            v4u z = {0, 0, 0, 0};
            kk[t] = r >= 0 ? *(const v4u*)(k + (size_t)r * D_ + ch0) : z;
        }
        const v4u vv = *(const v4u*)(v + (size_t)s * D_ + ch0);
        const v4u qq = *(const v4u*)(q + (size_t)s * D_ + ch0);
        v4u sw[4];
#pragma unroll
        for (int t = 0; t < 4; ++t) sw[t] = *(const v4u*)(sk + ch0 * 4 + 8 * t);
        const int srow = 8 * b + w;
        const v4u xr = *(const v4u*)(q + (size_t)srow * D_ + lane * 8);
        *(LAS v4u*)((LAS char*)smem + 8192 + row * 128 + c8 * 16) = qq;
        v4u o;
#pragma unroll
        for (int j = 0; j < 4; ++j) {
            float a = lo2f(kk[0][j]) * hi2f(sw[j].y);
            a = fmaf(lo2f(kk[1][j]), lo2f(sw[j].y), a);
            a = fmaf(lo2f(kk[2][j]), hi2f(sw[j].x), a);
            a = fmaf(lo2f(kk[3][j]), lo2f(sw[j].x), a);
            float bb = hi2f(kk[0][j]) * hi2f(sw[j].w);
            bb = fmaf(hi2f(kk[1][j]), lo2f(sw[j].w), bb);
            bb = fmaf(hi2f(kk[2][j]), hi2f(sw[j].z), bb);
            bb = fmaf(hi2f(kk[3][j]), lo2f(sw[j].z), bb);
            o[j] = pk2bf(a * lo2f(vv[j]), bb * hi2f(vv[j]));
        }
        *(LAS v4u*)((LAS char*)smem + row * 128 + c8 * 16) = o;
        float x[8];
#pragma unroll
        for (int j = 0; j < 4; ++j) { x[2 * j] = lo2f(xr[j]); x[2 * j + 1] = hi2f(xr[j]); }
        float sm = ((x[0] + x[1]) + (x[2] + x[3])) + ((x[4] + x[5]) + (x[6] + x[7]));
        sm = xadd32(xadd16(row16sum(sm)));
        const float mean = sm * (1.f / 512.f);
        float sq = 0.f;
#pragma unroll
        for (int j = 0; j < 8; ++j) sq = fmaf(x[j] - mean, x[j] - mean, sq);
        sq = xadd32(xadd16(row16sum(sq)));
        const float rs = __builtin_amdgcn_rsqf(sq * (1.f / 512.f) + 1e-5f);
        if (lane == 0) *(f32x2*)(ws + OFF_ST0 + srow * 8) = f32x2{rs, -mean * rs};
        STAMP(0, 1);
        __syncthreads();
        STAMP(0, 2);
        const int arr = w >> 2, cw = w & 3, qd = c >> 2, p = c & 3;
        u16* dst = (u16*)(ws + (arr ? OFF_X0T : OFF_UR)) + (size_t)(64 * cb + 16 * cw + c) * S_ + (arr ? 64 * tb : 1984 - 64 * tb);
#pragma unroll
        for (int i = 0; i < 2; ++i) {
            const int oc = 4 * i + g;
            const int r0 = arr ? 8 * oc + qd : 63 - 8 * oc - qd, r1 = arr ? r0 + 4 : r0 - 4;
            const LAS char* base = (const LAS char*)smem + arr * 8192 + 32 * cw + 8 * p;
            const v2u a = tr_read(base + r0 * 128), bb = tr_read(base + r1 * 128);
            *(v4u*)(dst + 8 * oc) = v4u{a.x, a.y, bb.x, bb.y};
        }
    } else if (b < 256 + NP) {
        const int p = b - 256;
        u32 d[2][8];
#pragma unroll
        for (int u = 0; u < 2; ++u)
#pragma unroll
            for (int e = 0; e < 8; ++e) {
                const int kr = 32 * (2 * w + u) + 16 * (e >> 2) + 4 * g + (e & 3);
                d[u][e] = *(const u32*)(w1 + (size_t)kr * F_ + 32 * p + 2 * c);
            }
        float s0 = 0.f, s1 = 0.f;
#pragma unroll
        for (int u = 0; u < 2; ++u) {
            v4u fa, fb;
#pragma unroll
            for (int i = 0; i < 4; ++i) {
                fa[i] = __builtin_amdgcn_perm(d[u][2 * i + 1], d[u][2 * i], 0x05040100u);
                fb[i] = __builtin_amdgcn_perm(d[u][2 * i + 1], d[u][2 * i], 0x07060302u);
            }
#pragma unroll
            for (int e = 0; e < 8; ++e) { s0 += lo2f(d[u][e]); s1 += hi2f(d[u][e]); }
            char* dst = ws + OFF_W1T + ((size_t)(2 * p) * 16 + 2 * w + u) * 1024 + lane * 16;
            *(v4u*)dst = fa;
            *(v4u*)(dst + 16384) = fb;
        }
        s0 = xadd32(xadd16(s0));
        s1 = xadd32(xadd16(s1));
        LAS float* red = (LAS float*)smem;
        if (g == 0) { red[w * 32 + c] = s0; red[w * 32 + 16 + c] = s1; }
        __syncthreads();
        if (tid < 32) {
            float t = 0.f;
#pragma unroll
            for (int i = 0; i < 8; ++i) t += red[i * 32 + tid];
            ((float*)(ws + OFF_CS))[32 * p + tid] = t;
        }
    } else {
        const int p = b - 256 - NP;
        v2u d[8];
#pragma unroll
        for (int e = 0; e < 8; ++e) {
            const int f = 32 * p + 2 * (4 * g + (e & 3)) + (e >> 2);
            d[e] = *(const v2u*)(w2 + (size_t)f * D_ + 64 * w + 4 * c);
        }
        char* dst = ws + OFF_W2T + ((size_t)(w * NP + p) * 4) * 1024 + lane * 16;
#pragma unroll
        for (int ct = 0; ct < 4; ++ct) {
            v4u f;
#pragma unroll
            for (int i = 0; i < 4; ++i)
                f[i] = __builtin_amdgcn_perm(d[2 * i + 1][ct >> 1], d[2 * i][ct >> 1], (ct & 1) ? 0x07060302u : 0x05040100u);
            *(v4u*)(dst + ct * 1024) = f;
        }
    }
    STAMP(0, 3);
    STAMPW(0, 4);
}

constexpr int CV_E = 0, CV_O = 4160, CV_U = 8320, CV_CH = 12928, CV_X = 2 * CV_CH, CV_BYTES = CV_X + 16384;

__global__ __launch_bounds__(256) void k_conv(const u16* __restrict__ ssmk, const u16* __restrict__ Dv, char* __restrict__ ws) {
    __shared__ __attribute__((aligned(16))) char smem_[CV_BYTES];
    LAS char* smem = (LAS char*)smem_;
    const int b = blockIdx.x, tid = threadIdx.x, lane = tid & 63, w = __builtin_amdgcn_readfirstlane(tid >> 6);
    const int c = lane & 15, g = lane >> 4;
    const int ch0 = 64 * (b & 7) + 2 * (b >> 3);
    const int cw = w >> 1, half = w & 1, ch = ch0 + cw;
    STAMP(1, 0);
    v4u hh[2], uu[2];
    u32 hn[2];
#pragma unroll
    for (int i = 0; i < 2; ++i) {
        uu[i] = *(const v4u*)(ws + OFF_UR + (size_t)(ch0 + i) * 4096 + tid * 16);
        hh[i] = *(const v4u*)(ssmk + (size_t)(ch0 + i) * S_ + tid * 8);
        hn[i] = tid < 255 ? *(const u32*)(ssmk + (size_t)(ch0 + i) * S_ + tid * 8 + 8) : 0u;
    }
    v2u x0[4];
    f32x4 sa[4], sb[4];
#pragma unroll
    for (int i = 0; i < 4; ++i) {
        const int s0 = 256 * (4 * half + i) + 16 * c + 4 * g;
        x0[i] = *(const v2u*)(ws + OFF_X0T + (size_t)ch * 4096 + s0 * 2);
        sa[i] = *(const f32x4*)(ws + OFF_ST0 + s0 * 8);
        sb[i] = *(const f32x4*)(ws + OFF_ST0 + s0 * 8 + 16);
    }
    const float dval = lo2f(Dv[ch]);
    SB();
    const v4u zero = {0, 0, 0, 0};
    if (tid < 60) {
        const int i = tid >= 30, r = tid - 30 * i;
        *(LAS v4u*)(smem + i * CV_CH + CV_U + 4096 + r * 16) = zero;
    } else if (tid >= 64 && tid < 70) {
        const int r = tid - 64, i = r >= 3, m = r - 3 * i;
        *(LAS v4u*)(smem + i * CV_CH + (m < 2 ? CV_E + 16 * m : CV_O)) = zero;
    }
#pragma unroll
    for (int i = 0; i < 2; ++i) {
        *(LAS v4u*)(smem + i * CV_CH + CV_U + tid * 16) = uu[i];
        *(LAS v4u*)(smem + i * CV_CH + CV_E + 32 + tid * 16) = hh[i];
        v4u o;
        o.x = __builtin_amdgcn_alignbyte(hh[i].y, hh[i].x, 2);
        o.y = __builtin_amdgcn_alignbyte(hh[i].z, hh[i].y, 2);
        o.z = __builtin_amdgcn_alignbyte(hh[i].w, hh[i].z, 2);
        o.w = __builtin_amdgcn_alignbyte(hn[i], hh[i].w, 2);
        *(LAS v4u*)(smem + i * CV_CH + CV_O + 32 + tid * 16) = o;
        if (tid == 0) *(LAS v4u*)(smem + i * CV_CH + CV_O + 16) = v4u{0, 0, 0, hh[i].x << 16};
    }
    STAMP(1, 1);
    __syncthreads();
    STAMP(1, 2);
    f32x4 acc[8];
#pragma unroll
    for (int i = 0; i < 8; ++i) acc[i] = f32x4{0.f, 0.f, 0.f, 0.f};
    const LAS char* ab = smem + cw * CV_CH + ((c & 1) ? CV_E + 2 * (1 + c) : CV_O + 2 * c) + 16 * g + 256 * half;
    const LAS char* bb = smem + cw * CV_CH + CV_U + 4064 - 3584 - 32 * c + 16 * g + 256 * half;
#pragma unroll
    for (int r = 0; r < 4; ++r) {
        v4u A[8], B[8];
#pragma unroll
        for (int i = 0; i < 8; ++i) {
            const LAS u32* pa = (const LAS u32*)(ab + 512 * i + 64 * r);
            A[i] = v4u{pa[0], pa[1], pa[2], pa[3]};
            B[i] = *(const LAS v4u*)(bb + 512 * (7 - i) + 64 * r);
        }
#pragma unroll
        for (int al = 0; al < 8; ++al)
#pragma unroll
            for (int be = 0; be + al < 8; ++be) acc[al + be] = mfma(A[al], B[be], acc[al + be]);
    }
    STAMP(1, 3);
    LAS f32x4* xch = (LAS f32x4*)(smem + CV_X);
    f32x4 fin[4];
    if (half) {
#pragma unroll
        for (int i = 0; i < 4; ++i) { xch[(w * 4 + i) * 64 + lane] = acc[i]; fin[i] = acc[4 + i]; }
    } else {
#pragma unroll
        for (int i = 0; i < 4; ++i) { xch[(w * 4 + i) * 64 + lane] = acc[4 + i]; fin[i] = acc[i]; }
    }
    __syncthreads();
#pragma unroll
    for (int i = 0; i < 4; ++i) {
        const int I = 4 * half + i, s0 = 256 * I + 16 * c + 4 * g;
        const f32x4 y = fin[i] + xch[((w ^ 1) * 4 + i) * 64 + lane];
        const v2u u = *(const LAS v2u*)(smem + cw * CV_CH + CV_U + 2 * (2044 - s0));
        const float y0 = fmaf(hi2f(u.y), dval, y[0]), y1 = fmaf(lo2f(u.y), dval, y[1]);
        const float y2 = fmaf(hi2f(u.x), dval, y[2]), y3 = fmaf(lo2f(u.x), dval, y[3]);
        const float a0 = lo2f(x0[i].x), a1 = hi2f(x0[i].x), a2 = lo2f(x0[i].y), a3 = hi2f(x0[i].y);
        const float z0 = fmaf(fmaf(a0, sa[i][0], sa[i][1]), y0, a0), z1 = fmaf(fmaf(a1, sa[i][2], sa[i][3]), y1, a1);
        const float z2 = fmaf(fmaf(a2, sb[i][0], sb[i][1]), y2, a2), z3 = fmaf(fmaf(a3, sb[i][2], sb[i][3]), y3, a3);
        const size_t off = (size_t)(4 * I + (c >> 2)) * 65536 + ch * 128 + (((c & 3) ^ ((ch >> 1) & 3)) << 5) + 8 * g;
        *(v2u*)(ws + OFF_Y + off) = v2u{pk2bf(y0, y1), pk2bf(y2, y3)};
        *(v2u*)(ws + OFF_X1 + off) = v2u{pk2bf(z0, z1), pk2bf(z2, z3)};
    }
    STAMP(1, 4);
    STAMPW(1, 5);
}

template <bool MIX>
__global__ __launch_bounds__(256) void k_g1(char* __restrict__ ws) {
    __shared__ __attribute__((aligned(16))) char smem_[65536 + 2048 + 512];
    LAS char* smem = (LAS char*)smem_;
    const int b = blockIdx.x, tid = threadIdx.x, lane = tid & 63, w = __builtin_amdgcn_readfirstlane(tid >> 6);
    const int c = lane & 15, g = lane >> 4;
    const int tb = 4 * (b & 7) + (b >> 6), cg = (b >> 3) & 7;
    const char* xsrc = ws + (MIX ? OFF_X2 : OFF_X1) + (size_t)tb * 65536 + w * 16384 + lane * 16;
    const char* ysrc = ws + OFF_Y + (size_t)tb * 65536 + w * 16384 + lane * 16;
    STAMP(MIX ? 4 : 2, 0);
    f32x2 pst[8];
    if (MIX) {
#pragma unroll
        for (int i = 0; i < 8; ++i) pst[i] = *(const f32x2*)(ws + OFF_ST2 + (tb * 8 + i) * 512 + lane * 8);
    }
    v4u xs[16], ys[16];
#pragma unroll
    for (int i = 0; i < 16; ++i) {
        xs[i] = *(const v4u*)(xsrc + i * 1024);
        if (MIX) ys[i] = *(const v4u*)(ysrc + i * 1024);
    }
    const int T0 = 11 * cg + 3 * w, Tend = cg == 7 ? NT : 11 * cg + 11;
    const __amdgpu_buffer_rsrc_t rw = __builtin_amdgcn_make_buffer_rsrc(ws + OFF_W1T, 0, NT * 16384, 0x00020000);
    int vo[3];
#pragma unroll
    for (int j = 0; j < 3; ++j) vo[j] = T0 + j < Tend ? (T0 + j) * 16384 + lane * 16 : 0x40000000;
    v4u W[16][3];
    constexpr int KE = MIX ? 7 : 14;
#pragma unroll
    for (int kk = 0; kk < KE; ++kk)
#pragma unroll
        for (int j = 0; j < 3; ++j) W[kk][j] = __builtin_amdgcn_raw_buffer_load_b128(rw, vo[j], kk * 1024, 0);
    SB();
    if (MIX) {
        float sm = 0.f, sq = 0.f;
#pragma unroll
        for (int i = 0; i < 8; ++i) { sm += pst[i].x; sq += pst[i].y; }
        const float mean = sm * (1.f / 512.f);
        const float rs = __builtin_amdgcn_rsqf(fmaxf(sq * (1.f / 512.f) - mean * mean, 0.f) + 1e-5f);
        LAS f32x2* st = (LAS f32x2*)(smem + 65536 + w * 512);
        st[lane] = f32x2{rs, -mean * rs};
        const int tok0 = 16 * (((lane & 7) >> 1) ^ g) + 8 * (lane & 1);
        f32x4 ab[4];
#pragma unroll
        for (int i = 0; i < 4; ++i) ab[i] = *(LAS f32x4*)(st + tok0 + 2 * i);
        const bool keep = (cg >> 1) == w;
        char* x3 = ws + OFF_X3 + (size_t)tb * 65536 + w * 16384 + lane * 16;
#pragma unroll
        for (int i = 0; i < 16; ++i) {
            v4u o;
#pragma unroll
            for (int j = 0; j < 4; ++j) {
                const float x0 = lo2f(xs[i][j]), x1 = hi2f(xs[i][j]);
                o[j] = pk2bf(fmaf(fmaf(x0, ab[j][0], ab[j][1]), lo2f(ys[i][j]), x0),
                             fmaf(fmaf(x1, ab[j][2], ab[j][3]), hi2f(ys[i][j]), x1));
            }
            *(LAS v4u*)(smem + w * 16384 + i * 1024 + lane * 16) = o;
            if (keep && (i >> 3) == (cg & 1)) *(v4u*)(x3 + i * 1024) = o;
            if (i < 9) {
#pragma unroll
                for (int j = 0; j < 3; ++j) W[KE + i][j] = __builtin_amdgcn_raw_buffer_load_b128(rw, vo[j], (KE + i) * 1024, 0);
            }
            SB();
        }
    } else {
#pragma unroll
        for (int i = 0; i < 16; ++i) {
            *(LAS v4u*)(smem + w * 16384 + i * 1024 + lane * 16) = xs[i];
            if (i >= 3 && i < 5) {
#pragma unroll
                for (int j = 0; j < 3; ++j) W[KE + i - 3][j] = __builtin_amdgcn_raw_buffer_load_b128(rw, vo[j], (KE + i - 3) * 1024, 0);
            }
            SB();
        }
    }
    f32x4 cs[3];
#pragma unroll
    for (int j = 0; j < 3; ++j) cs[j] = *(const f32x4*)(ws + OFF_CS + (T0 + j) * 64 + g * 16);
    STAMP(MIX ? 4 : 2, 1);
    __syncthreads();
    STAMP(MIX ? 4 : 2, 2);
    const LAS char* xb[4];
#pragma unroll
    for (int i = 0; i < 4; ++i)
        xb[i] = smem + (4 * g + (c >> 2)) * 128 + ((((i + w) & 3) ^ ((2 * g + (c >> 3)) & 3)) << 5) + 8 * (c & 3);
    f32x4 acc[3][4];
#pragma unroll
    for (int j = 0; j < 3; ++j)
#pragma unroll
        for (int i = 0; i < 4; ++i) acc[j][i] = f32x4{0.f, 0.f, 0.f, 0.f};
    float sm = 0.f, sq = 0.f;
#pragma unroll
    for (int kk = 0; kk < 16; ++kk) {
        v4u X[4];
#pragma unroll
        for (int i = 0; i < 4; ++i) {
            const v2u lo = tr_read(xb[i] + kk * 4096), hi = tr_read(xb[i] + kk * 4096 + 2048);
            X[i] = v4u{lo.x, lo.y, hi.x, hi.y};
        }
#pragma unroll
        for (int j = 0; j < 3; ++j)
#pragma unroll
            for (int i = 0; i < 4; ++i) acc[j][i] = mfma(W[kk][j], X[i], acc[j][i]);
#pragma unroll
        for (int e = 0; e < 4; ++e) { sm = dot2(X[0][e], 0x3f803f80u, sm); sq = dot2(X[0][e], X[0][e], sq); }
    }
    STAMP(MIX ? 4 : 2, 3);
    sm = xadd32(xadd16(sm));
    sq = xadd32(xadd16(sq));
    {
        const float mean = sm * (1.f / 512.f);
        const float rs = __builtin_amdgcn_rsqf(fmaxf(sq * (1.f / 512.f) - mean * mean, 0.f) + 1e-5f);
        if (g == 0) *(LAS f32x2*)(smem + 65536 + 2048 + (w * 16 + c) * 8) = f32x2{rs, -mean * rs};
    }
    __syncthreads();
#pragma unroll
    for (int i = 0; i < 4; ++i) {
        const int tt = (i + w) & 3;
        const f32x2 ab = *(LAS f32x2*)(smem + 65536 + 2048 + (tt * 16 + c) * 8);
#pragma unroll
        for (int j = 0; j < 3; ++j) {
            const int T = T0 + j;
            const f32x4 u = acc[j][i] * ab.x + cs[j] * ab.y;
            const s16x2 z = {0, 0};
            const v2u o = {__builtin_bit_cast(u32, __builtin_elementwise_max(__builtin_bit_cast(s16x2, pk2bf(u[0], u[1])), z)),
                           __builtin_bit_cast(u32, __builtin_elementwise_max(__builtin_bit_cast(s16x2, pk2bf(u[2], u[3])), z))};
            if (T < Tend) *(v2u*)(ws + OFF_H + ((size_t)(tb * NP + (T >> 1)) * 4 + tt) * 1024 + lane * 16 + 8 * (T & 1)) = o;
        }
    }
    STAMP(MIX ? 4 : 2, 4);
    STAMPW(MIX ? 4 : 2, 5);
}

template <bool FINAL>
__global__ __launch_bounds__(256) void k_g2(char* __restrict__ ws, u16* __restrict__ out) {
    __shared__ __attribute__((aligned(16))) char smem_[49152];
    LAS char* smem = (LAS char*)smem_;
    const int b = blockIdx.x, tid = threadIdx.x, lane = tid & 63, w = __builtin_amdgcn_readfirstlane(tid >> 6);
    const int c = lane & 15, g = lane >> 4;
    const int tb = 4 * (b & 7) + (b >> 6), cg = (b >> 3) & 7;
    const __amdgpu_buffer_rsrc_t rh = __builtin_amdgcn_make_buffer_rsrc(ws + OFF_H + (size_t)tb * NP * 4096, 0, NP * 4096, 0x00020000);
    const __amdgpu_buffer_rsrc_t rw = __builtin_amdgcn_make_buffer_rsrc(ws + OFF_W2T + (size_t)cg * NP * 4096, 0, NP * 4096, 0x00020000);
    STAMP(FINAL ? 5 : 3, 0);
    int ho[4];
#pragma unroll
    for (int i = 0; i < 4; ++i) ho[i] = w * 4096 + ((i + w) & 3) * 1024 + lane * 16;
    const int wo = w * 4096 + lane * 16;
    const int inv = w == 3 ? 0x40000000 : 0;
    v4u H[11][4], W[11][4];
    constexpr int PRE = 7;
#define G2_LOAD(s)                                                                                              \
    {                                                                                                           \
        _Pragma("unroll") for (int i = 0; i < 4; ++i) {                                                         \
            H[s][i] = __builtin_amdgcn_raw_buffer_load_b128(rh, ho[i] + ((s) == 10 ? inv : 0), (s) * 16384, 0); \
            W[s][i] = __builtin_amdgcn_raw_buffer_load_b128(rw, wo + i * 1024 + ((s) == 10 ? inv : 0), (s) * 16384, 0); \
        }                                                                                                       \
    }
#pragma unroll
    for (int s = 0; s < PRE; ++s) G2_LOAD(s)
    v2u res[4];
#pragma unroll
    for (int ct = 0; ct < 4; ++ct) {
        const int ch = 64 * cg + 4 * c + ct;
        res[ct] = *(const v2u*)(ws + (FINAL ? OFF_X3 : OFF_X1) + (size_t)tb * 65536 + ch * 128 + ((w ^ ((ch >> 1) & 3)) << 5) + 8 * g);
    }
    SB();
    f32x4 acc[4][4];
#pragma unroll
    for (int i = 0; i < 4; ++i)
#pragma unroll
        for (int ct = 0; ct < 4; ++ct) acc[i][ct] = f32x4{0.f, 0.f, 0.f, 0.f};
#pragma unroll
    for (int s = 0; s < 11; ++s) {
#pragma unroll
        for (int i = 0; i < 4; ++i)
#pragma unroll
            for (int ct = 0; ct < 4; ++ct) acc[i][ct] = mfma(H[s][i], W[s][ct], acc[i][ct]);
        SB();
        if (s + PRE < 11) G2_LOAD(s + PRE)
        SB();
    }
    STAMP(FINAL ? 5 : 3, 1);
    LAS f32x4* red = (LAS f32x4*)smem;
#pragma unroll
    for (int i = 1; i < 4; ++i)
#pragma unroll
        for (int ct = 0; ct < 4; ++ct) red[((((i + w) & 3) * 3 + (i - 1)) * 4 + ct) * 64 + lane] = acc[i][ct];
    __syncthreads();
    STAMP(FINAL ? 5 : 3, 2);
    u32 xo[4][2];
    float xf[4][4];
#pragma unroll
    for (int ct = 0; ct < 4; ++ct) {
        const f32x4 t = (acc[0][ct] + red[((w * 3 + 0) * 4 + ct) * 64 + lane]) +
                        (red[((w * 3 + 1) * 4 + ct) * 64 + lane] + red[((w * 3 + 2) * 4 + ct) * 64 + lane]);
        xf[ct][0] = lo2f(res[ct].x) + t[0];
        xf[ct][1] = hi2f(res[ct].x) + t[1];
        xf[ct][2] = lo2f(res[ct].y) + t[2];
        xf[ct][3] = hi2f(res[ct].y) + t[3];
    }
    if (FINAL) {
#pragma unroll
        for (int r = 0; r < 4; ++r)
            *(v2u*)(out + (size_t)(64 * tb + 16 * w + 4 * g + r) * D_ + 64 * cg + 4 * c) =
                v2u{pk2bf(xf[0][r], xf[1][r]), pk2bf(xf[2][r], xf[3][r])};
    } else {
        float sm[4] = {0.f, 0.f, 0.f, 0.f}, sq[4] = {0.f, 0.f, 0.f, 0.f};
#pragma unroll
        for (int ct = 0; ct < 4; ++ct) {
            const int ch = 64 * cg + 4 * c + ct;
            xo[ct][0] = pk2bf(xf[ct][0], xf[ct][1]);
            xo[ct][1] = pk2bf(xf[ct][2], xf[ct][3]);
            *(v2u*)(ws + OFF_X2 + (size_t)tb * 65536 + ch * 128 + ((w ^ ((ch >> 1) & 3)) << 5) + 8 * g) = v2u{xo[ct][0], xo[ct][1]};
            const float x0 = lo2f(xo[ct][0]), x1 = hi2f(xo[ct][0]), x2 = lo2f(xo[ct][1]), x3 = hi2f(xo[ct][1]);
            sm[0] += x0; sm[1] += x1; sm[2] += x2; sm[3] += x3;
            sq[0] = fmaf(x0, x0, sq[0]); sq[1] = fmaf(x1, x1, sq[1]); sq[2] = fmaf(x2, x2, sq[2]); sq[3] = fmaf(x3, x3, sq[3]);
        }
#pragma unroll
        for (int r = 0; r < 4; ++r) { sm[r] = row16sum(sm[r]); sq[r] = row16sum(sq[r]); }
        if (c == 0) {
            char* st = ws + OFF_ST2 + (tb * 8 + cg) * 512 + (16 * w + 4 * g) * 8;
            *(f32x4*)st = f32x4{sm[0], sq[0], sm[1], sq[1]};
            *(f32x4*)(st + 16) = f32x4{sm[2], sq[2], sm[3], sq[3]};
        }
    }
    STAMP(FINAL ? 5 : 3, 3);
    STAMPW(FINAL ? 5 : 3, 4);
}

extern "C" void kernel_launch(void* const* d_in, const int* in_sizes, int n_in, void* d_out, int out_size, void* d_ws,
                              size_t ws_size, hipStream_t stream) {
    const u16* q = (const u16*)d_in[0];
    const u16* k = (const u16*)d_in[1];
    const u16* v = (const u16*)d_in[2];
    const u16* sk = (const u16*)d_in[3];
    const u16* ssmk = (const u16*)d_in[4];
    const u16* Dv = (const u16*)d_in[5];
    const u16* w1 = (const u16*)d_in[7];
    const u16* w2 = (const u16*)d_in[8];
    char* ws = (char*)d_ws;
    k_prep<<<256 + 2 * NP, 512, 0, stream>>>(q, k, v, sk, w1, w2, ws);
    k_conv<<<256, 256, 0, stream>>>(ssmk, Dv, ws);
    k_g1<false><<<256, 256, 0, stream>>>(ws);
    k_g2<false><<<256, 256, 0, stream>>>(ws, (u16*)d_out);
    k_g1<true><<<256, 256, 0, stream>>>(ws);
    k_g2<true><<<256, 256, 0, stream>>>(ws, (u16*)d_out);
}
```

```cpp
#include <hip/hip_runtime.h>
#include <hip/hip_bf16.h>
#include <stdint.h>

typedef unsigned short u16;
typedef unsigned int u32;
typedef unsigned long long u64;
typedef u32 v4u __attribute__((ext_vector_type(4)));
typedef u32 v2u __attribute__((ext_vector_type(2)));
typedef float f32x4 __attribute__((ext_vector_type(4)));
typedef float f32x2 __attribute__((ext_vector_type(2)));
typedef short bf16x8 __attribute__((ext_vector_type(8)));
typedef short s16x4 __attribute__((ext_vector_type(4)));
typedef short s16x2 __attribute__((ext_vector_type(2)));
typedef __bf16 bf16x2 __attribute__((ext_vector_type(2)));
#define LAS __attribute__((address_space(3)))
#define GAS __attribute__((address_space(1)))
#define DEV __device__ __forceinline__
#define RLX __ATOMIC_RELAXED, __HIP_MEMORY_SCOPE_AGENT
typedef __amdgpu_buffer_rsrc_t rsrc_t;

constexpr int S_ = 2048, D_ = 512, F_ = 1376;
constexpr int NP = 43;
constexpr int NT = 86;
constexpr int MB = 1 << 20;
constexpr int OFF_FL = 0, OFF_ST0 = 512 * 1024, OFF_ST2 = 640 * 1024, OFF_CS = 960 * 1024;
constexpr int OFF_UR = 1 * MB, OFF_X0T = 3 * MB, OFF_Y = 5 * MB, OFF_X1 = 7 * MB, OFF_X2 = 9 * MB, OFF_W1T = 11 * MB,
              OFF_W2T = 13 * MB, OFF_H = 15 * MB, H_LAYER = 6 * MB, WS_USED = 27 * MB;
constexpr int FL_EPOCH = 0, FL_A2 = 1024, FL_A = 4096, FL_B = 40960, FL_G = 81920;
constexpr int SC1 = 16, OOB = 0x40000000;
constexpr int L_X = 0, L_Y = 65536, L_RED = 131072, L_ST = 155648, L_MS = L_ST + 1024, L_CS = L_MS + 1024, L_BYTES = L_CS + 1024;
constexpr int CV_E = 0, CV_O = 4160, CV_U = 8320, CV_CH = 12928, CV_X = 2 * CV_CH, L_P0 = 49152;
constexpr u32 SPIN_CAP = 1u << 18;

DEV float lo2f(u32 d) { return __builtin_bit_cast(float, d << 16); }
DEV float hi2f(u32 d) { return __builtin_bit_cast(float, d & 0xffff0000u); }
DEV u32 pk2bf(float a, float b) {
    f32x2 x = {a, b};
    return __builtin_bit_cast(u32, __builtin_convertvector(x, bf16x2));
}
DEV f32x4 mfma(v4u a, v4u b, f32x4 c) {
    return __builtin_amdgcn_mfma_f32_16x16x32_bf16(__builtin_bit_cast(bf16x8, a), __builtin_bit_cast(bf16x8, b), c, 0, 0, 0);
}
DEV v2u tr_read(const LAS char* p) {
    return __builtin_bit_cast(v2u, __builtin_amdgcn_ds_read_tr16_b64_v4i16((LAS s16x4*)p));
}
DEV float dot2(u32 a, u32 b, float c) {
    return __builtin_amdgcn_fdot2_f32_bf16(__builtin_bit_cast(bf16x2, a), __builtin_bit_cast(bf16x2, b), c, false);
}
DEV float xadd16(float v) {
    auto r = __builtin_amdgcn_permlane16_swap(__builtin_bit_cast(u32, v), __builtin_bit_cast(u32, v), false, false);
    const u32 r0 = r[0], r1 = r[1];
    return __builtin_bit_cast(float, r0) + __builtin_bit_cast(float, r1);
}
DEV float xadd32(float v) {
    auto r = __builtin_amdgcn_permlane32_swap(__builtin_bit_cast(u32, v), __builtin_bit_cast(u32, v), false, false);
    const u32 r0 = r[0], r1 = r[1];
    return __builtin_bit_cast(float, r0) + __builtin_bit_cast(float, r1);
}
template <int CTRL> DEV float dppadd(float v) {
    return v + __builtin_bit_cast(float, __builtin_amdgcn_update_dpp(0, __builtin_bit_cast(int, v), CTRL, 0xf, 0xf, true));
}
DEV float row16sum(float v) {
    v = dppadd<0x121>(v);
    v = dppadd<0x122>(v);
    v = dppadd<0x124>(v);
    v = dppadd<0x128>(v);
    return v;
}
DEV rsrc_t mkrs(const void* p, int bytes) { return __builtin_amdgcn_make_buffer_rsrc((void*)p, 0, bytes, 0x00020000); }
#define LD16(r, vo, so) __builtin_amdgcn_raw_buffer_load_b128(r, vo, so, 0)
#define LD16S(r, vo, so) __builtin_amdgcn_raw_buffer_load_b128(r, vo, so, SC1)
#define LD8S(r, vo, so) __builtin_amdgcn_raw_buffer_load_b64(r, vo, so, SC1)
#define ST16S(v, r, vo, so) __builtin_amdgcn_raw_buffer_store_b128(v, r, vo, so, SC1)
#define ST8S(v, r, vo, so) __builtin_amdgcn_raw_buffer_store_b64(v, r, vo, so, SC1)
#define SB() __builtin_amdgcn_sched_barrier(0)
#define PIN_A(x) asm volatile("" : "+a"(x))
#define PIN_V(x) asm volatile("" : "+v"(x))
#define BAR() do { SB(); asm volatile("s_waitcnt lgkmcnt(0)\n\ts_barrier" ::: "memory"); SB(); } while (0)
#define VMW(n) do { SB(); asm volatile("s_waitcnt vmcnt(" #n ")" ::: "memory"); SB(); } while (0)
DEV void glds4(const char* sbase, u32 voff, u32 lds) {
    u32 keep;
    asm volatile(
        "s_mov_b32 %0, m0\n\ts_mov_b32 m0, %3\n\ts_nop 0\n\t"
        "global_load_lds_dwordx4 %1, %2 sc1\n\t"
        "global_load_lds_dwordx4 %1, %2 offset:1024 sc1\n\t"
        "global_load_lds_dwordx4 %1, %2 offset:2048 sc1\n\t"
        "global_load_lds_dwordx4 %1, %2 offset:3072 sc1\n\t"
        "s_mov_b32 m0, %0"
        : "=&s"(keep) : "v"(voff), "s"(sbase), "s"(lds) : "memory");
}
DEV void poll64(const char* p, int stride, int n, u64 want, int lane, bool& dead) {
    const GAS u64* a = (const GAS u64*)(p + (lane < n ? lane : 0) * stride);
    if (!dead) {
        u32 spins = 0;
        while (!__all(__hip_atomic_load(a, RLX) == want)) {
            if (++spins > SPIN_CAP) { dead = true; break; }
        }
    }
    __builtin_amdgcn_fence(__ATOMIC_ACQUIRE, "wavefront");
}

#define STAMP(i)

__global__ __launch_bounds__(256, 1) void k_all(const u16* __restrict__ q, const u16* __restrict__ k, const u16* __restrict__ v,
                                                const u16* __restrict__ sk, const u16* __restrict__ ssmk,
                                                const u16* __restrict__ Dv, const u16* __restrict__ w1,
                                                const u16* __restrict__ w2, u16* __restrict__ out, char* __restrict__ ws) {
    extern __shared__ __attribute__((aligned(16))) char smem_[];
    LAS char* smem = (LAS char*)smem_;
    const int b = blockIdx.x, tid = threadIdx.x, lane = tid & 63, w = __builtin_amdgcn_readfirstlane(tid >> 6);
    const int c = lane & 15, g = lane >> 4;
    const int xc = b & 7, jb = b >> 3;
    const rsrc_t rws = mkrs(ws, WS_USED);
    bool dead = false;
    STAMP(0);
    const u64 epoch = __hip_atomic_load((const GAS u64*)(ws + OFF_FL + FL_EPOCH), RLX);
    const int ch0 = 64 * xc + 2 * jb;
    {
        const int row = 2 * (tid >> 3), c8 = tid & 7, s0 = 64 * jb + row;
        const int vo = s0 * 1024 + xc * 128 + c8 * 16;
        const rsrc_t rk = mkrs(k, 2 * MB), rv = mkrs(v, 2 * MB), rq = mkrs(q, 2 * MB);
        v4u kk[5], vv[2], qq[2], sw[4], xr[2], hh[2];
        u32 hn[2];
#pragma unroll
        for (int t = 0; t < 5; ++t) kk[t] = LD16(rk, s0 + t - 3 >= 0 ? vo + (t - 3) * 1024 : OOB, 0);
#pragma unroll
        for (int t = 0; t < 2; ++t) vv[t] = LD16(rv, vo + t * 1024, 0);
#pragma unroll
        for (int t = 0; t < 4; ++t) sw[t] = *(const v4u*)(sk + (64 * xc + 8 * c8) * 4 + 8 * t);
#pragma unroll
        for (int t = 0; t < 2; ++t) qq[t] = LD16(rq, vo + t * 1024, 0);
#pragma unroll
        for (int i = 0; i < 2; ++i) {
            hh[i] = *(const v4u*)(ssmk + (size_t)(ch0 + i) * S_ + tid * 8);
            hn[i] = *(const u32*)(ssmk + (size_t)(ch0 + i) * S_ + (tid < 255 ? tid * 8 + 8 : 0));
            xr[i] = LD16(rq, (8 * b + 2 * w + i) * 1024 + lane * 16, 0);
        }
        u32 wd[16];
        if (b < 2 * NP) {
            const int p = b >> 1;
#pragma unroll
            for (int e = 0; e < 16; ++e) {
                const int kr = 32 * (8 * (b & 1) + 2 * w + (e >> 3)) + 16 * ((e >> 2) & 1) + 4 * g + (e & 3);
                wd[e] = *(const u32*)(w1 + (size_t)kr * F_ + 32 * p + 2 * c);
            }
        } else if (b < 4 * NP) {
            const int p = (b - 2 * NP) >> 1, cgw = 4 * (b & 1) + w;
#pragma unroll
            for (int e = 0; e < 16; ++e) {
                const int f = 32 * p + 2 * (4 * g + (e & 3)) + ((e >> 2) & 1);
                wd[e] = *(const u32*)(w2 + (size_t)f * D_ + 64 * cgw + 32 * (e >> 3) + 2 * c);
            }
        }
        SB();
        if (tid == 0) __hip_atomic_store((GAS u32*)(ws + OFF_FL + FL_B + b * 128), 0u, RLX);
#pragma unroll
        for (int t = 0; t < 2; ++t) {
            v4u o;
#pragma unroll
            for (int j = 0; j < 4; ++j) {
                float a = lo2f(kk[t][j]) * hi2f(sw[j].y);
                a = fmaf(lo2f(kk[t + 1][j]), lo2f(sw[j].y), a);
                a = fmaf(lo2f(kk[t + 2][j]), hi2f(sw[j].x), a);
                a = fmaf(lo2f(kk[t + 3][j]), lo2f(sw[j].x), a);
                float bb = hi2f(kk[t][j]) * hi2f(sw[j].w);
                bb = fmaf(hi2f(kk[t + 1][j]), lo2f(sw[j].w), bb);
                bb = fmaf(hi2f(kk[t + 2][j]), hi2f(sw[j].z), bb);
                bb = fmaf(hi2f(kk[t + 3][j]), lo2f(sw[j].z), bb);
                o[j] = pk2bf(a * lo2f(vv[t][j]), bb * hi2f(vv[t][j]));
            }
            *(LAS v4u*)(smem + L_P0 + (row + t) * 128 + c8 * 16) = o;
            *(LAS v4u*)(smem + L_P0 + 8192 + (row + t) * 128 + c8 * 16) = qq[t];
        }
        BAR();
        STAMP(1);
        {
            const LAS char* base = smem + L_P0 + 32 * w + 8 * (c & 3);
            const int qd = c >> 2, dst = (64 * xc + 16 * w + c) * 4096;
#pragma unroll
            for (int i = 0; i < 2; ++i) {
                const int oc = 4 * i + g;
                const v2u a = tr_read(base + (63 - 8 * oc - qd) * 128), bb = tr_read(base + (59 - 8 * oc - qd) * 128);
                ST16S((v4u{a.x, a.y, bb.x, bb.y}), rws, dst + (1984 - 64 * jb + 8 * oc) * 2, OFF_UR);
            }
#pragma unroll
            for (int i = 0; i < 2; ++i) {
                const int oc = 4 * i + g;
                const v2u a = tr_read(base + 8192 + (8 * oc + qd) * 128), bb = tr_read(base + 8192 + (8 * oc + 4 + qd) * 128);
                ST16S((v4u{a.x, a.y, bb.x, bb.y}), rws, dst + (64 * jb + 8 * oc) * 2, OFF_X0T);
            }
        }
        f32x4 st;
#pragma unroll
        for (int i = 0; i < 2; ++i) {
            float x[8];
#pragma unroll
            for (int j = 0; j < 4; ++j) { x[2 * j] = lo2f(xr[i][j]); x[2 * j + 1] = hi2f(xr[i][j]); }
            float sm = ((x[0] + x[1]) + (x[2] + x[3])) + ((x[4] + x[5]) + (x[6] + x[7]));
            sm = xadd32(xadd16(row16sum(sm)));
            const float mean = sm * (1.f / 512.f);
            float sq = 0.f;
#pragma unroll
            for (int j = 0; j < 8; ++j) sq = fmaf(x[j] - mean, x[j] - mean, sq);
            sq = xadd32(xadd16(row16sum(sq)));
            const float rs = __builtin_amdgcn_rsqf(sq * (1.f / 512.f) + 1e-5f);
            st[2 * i] = rs;
            st[2 * i + 1] = -mean * rs;
        }
        ST16S(__builtin_bit_cast(v4u, st), rws, lane == 0 ? (8 * b + 2 * w) * 8 : OOB, OFF_ST0);
        if (b < 4 * NP) {
            v4u fr[4];
#pragma unroll
            for (int u = 0; u < 2; ++u)
#pragma unroll
                for (int i = 0; i < 4; ++i) {
                    fr[2 * u][i] = __builtin_amdgcn_perm(wd[8 * u + 2 * i + 1], wd[8 * u + 2 * i], 0x05040100u);
                    fr[2 * u + 1][i] = __builtin_amdgcn_perm(wd[8 * u + 2 * i + 1], wd[8 * u + 2 * i], 0x07060302u);
                }
            const bool is1 = b < 2 * NP;
            const int p = is1 ? b >> 1 : (b - 2 * NP) >> 1;
            const int base = is1 ? OFF_W1T + (2 * p * 16 + 8 * (b & 1) + 2 * w) * 1024 : OFF_W2T + ((4 * (b & 1) + w) * NP + p) * 4096;
            const int su = is1 ? 1024 : 2048, sp = is1 ? 16384 : 1024;
#pragma unroll
            for (int u = 0; u < 4; ++u) ST16S(fr[u], rws, lane * 16, base + (u >> 1) * su + (u & 1) * sp);
            if (is1) {
                float s0 = 0.f, s1 = 0.f;
#pragma unroll
                for (int e = 0; e < 16; ++e) { s0 += lo2f(wd[e]); s1 += hi2f(wd[e]); }
                s0 = xadd32(xadd16(s0));
                s1 = xadd32(xadd16(s1));
                __builtin_amdgcn_raw_buffer_store_b32(__builtin_bit_cast(u32, g == 0 ? s0 : s1), rws,
                                                      g < 2 ? ((4 * (b & 1) + w) * F_ + 32 * p + lane) * 4 : OOB, OFF_CS, SC1);
            }
        }
        const v4u zero = {0, 0, 0, 0};
        if (tid < 60) {
            const int i = tid >= 30, r = tid - 30 * i;
            *(LAS v4u*)(smem + i * CV_CH + CV_U + 4096 + r * 16) = zero;
        } else if (tid >= 64 && tid < 70) {
            const int r = tid - 64, i = r >= 3, m = r - 3 * i;
            *(LAS v4u*)(smem + i * CV_CH + (m < 2 ? CV_E + 16 * m : CV_O)) = zero;
        }
#pragma unroll
        for (int i = 0; i < 2; ++i) {
            *(LAS v4u*)(smem + i * CV_CH + CV_E + 32 + tid * 16) = hh[i];
            v4u o;
            o.x = __builtin_amdgcn_alignbyte(hh[i].y, hh[i].x, 2);
            o.y = __builtin_amdgcn_alignbyte(hh[i].z, hh[i].y, 2);
            o.z = __builtin_amdgcn_alignbyte(hh[i].w, hh[i].z, 2);
            o.w = __builtin_amdgcn_alignbyte(tid < 255 ? hn[i] : 0u, hh[i].w, 2);
            *(LAS v4u*)(smem + i * CV_CH + CV_O + 32 + tid * 16) = o;
            if (tid == 0) *(LAS v4u*)(smem + i * CV_CH + CV_O + 16) = v4u{0, 0, 0, hh[i].x << 16};
        }
    }
    const u64 tag = epoch * 6364136223846793005ull + 1442695040888963407ull;
    STAMP(2);
    VMW(0);
    BAR();
    STAMP(3);
    if (tid < 2) __hip_atomic_store((GAS u64*)(ws + OFF_FL + (tid ? FL_A2 + b * 8 : FL_A + (xc * 32 + jb) * 128)), tag, RLX);
    if (w == 0) poll64(ws + OFF_FL + FL_A + xc * 4096, 128, 32, tag, lane, dead);
    BAR();
    STAMP(4);
    const int grp = 4 * xc + (b >> 6), cg = jb & 7;
    const int T0 = 11 * cg + 3 * w, Tend = cg == 7 ? NT : 11 * cg + 11;
    v4u W1[16][3], W2[11][4];
    {
        const int cw = w >> 1, half = w & 1, ch = ch0 + cw;
        {
            u32 keep;
            asm volatile(
                "s_mov_b32 %0, m0\n\ts_mov_b32 m0, %3\n\ts_nop 0\n\t"
                "global_load_lds_dwordx4 %1, %2 sc1\n\t"
                "global_load_lds_dwordx4 %1, %2 offset:1024 sc1\n\t"
                "s_mov_b32 m0, %0"
                : "=&s"(keep) : "v"(lane * 16), "s"(ws + OFF_UR + ch * 4096 + half * 2048),
                  "s"((u32)(size_t)(smem + cw * CV_CH + CV_U + half * 2048)) : "memory");
        }
        v4u fa0 = LD16S(rws, lane * 32, OFF_FL + FL_A2), fa1 = LD16S(rws, lane * 32 + 16, OFF_FL + FL_A2);
        const float dval = lo2f(Dv[ch]);
        VMW(3);
        BAR();
        STAMP(5);
        {
            const u32 tl = (u32)tag, th = (u32)(tag >> 32);
            u32 spins = 0;
            while (!__all(fa0.x == tl && fa0.z == tl && fa1.x == tl && fa1.z == tl && fa0.y == th && fa0.w == th && fa1.y == th && fa1.w == th)) {
                if (dead || ++spins > SPIN_CAP) { dead = true; break; }
                fa0 = LD16S(rws, lane * 32, OFF_FL + FL_A2);
                fa1 = LD16S(rws, lane * 32 + 16, OFF_FL + FL_A2);
            }
            __builtin_amdgcn_fence(__ATOMIC_ACQUIRE, "wavefront");
        }
        STAMP(38);
        v2u x0[4];
        f32x4 sa[4], sb[4];
#pragma unroll
        for (int i = 0; i < 4; ++i) {
            const int s0 = 256 * (4 * half + i) + 16 * c + 4 * g;
            x0[i] = LD8S(rws, ch * 4096 + s0 * 2, OFF_X0T);
            sa[i] = __builtin_bit_cast(f32x4, LD16S(rws, s0 * 8, OFF_ST0));
            sb[i] = __builtin_bit_cast(f32x4, LD16S(rws, s0 * 8 + 16, OFF_ST0));
        }
        {
            int vo[3];
#pragma unroll
            for (int j = 0; j < 3; ++j) vo[j] = T0 + j < Tend ? (T0 + j) * 16384 + lane * 16 : OOB;
            const int rot = 8 * (w >> 1);
#pragma unroll
            for (int kk = 0; kk < 16; ++kk)
#pragma unroll
                for (int j = 0; j < 3; ++j) W1[kk][j] = LD16S(rws, vo[j], OFF_W1T + ((kk + rot) & 15) * 1024);
        }
        SB();
        STAMP(39);
        f32x4 acc[8];
#pragma unroll
        for (int i = 0; i < 8; ++i) acc[i] = f32x4{0.f, 0.f, 0.f, 0.f};
        const LAS char* ab = smem + cw * CV_CH + ((c & 1) ? CV_E + 2 * (1 + c) : CV_O + 2 * c) + 16 * g + 256 * half;
        const LAS char* bb = smem + cw * CV_CH + CV_U + 4064 - 3584 - 32 * c + 16 * g + 256 * half;
        v4u A[2][8], B[2][8];
#define CV_LOAD(buf, r)                                                           \
    _Pragma("unroll") for (int i = 0; i < 8; ++i) {                               \
        const LAS u32* pa = (const LAS u32*)(ab + 512 * i + 64 * (r));            \
        B[buf][i] = *(const LAS v4u*)(bb + 512 * (7 - i) + 64 * (r));             \
        A[buf][i] = v4u{pa[0], pa[1], pa[2], pa[3]};                              \
    }
        CV_LOAD(0, 0)
#pragma unroll
        for (int r = 0; r < 4; ++r) {
            SB();
            if (r < 3) CV_LOAD((r + 1) & 1, r + 1)
#pragma unroll
            for (int al = 0; al < 8; ++al)
#pragma unroll
                for (int be = 0; be + al < 8; ++be) acc[al + be] = mfma(A[r & 1][al], B[r & 1][be], acc[al + be]);
            if (r < 3) {
#pragma unroll
                for (int i = 0; i < 24; ++i) {
                    __builtin_amdgcn_sched_group_barrier(0x8, 1, 0);
                    __builtin_amdgcn_sched_group_barrier(0x100, 1, 0);
                }
            }
        }
        STAMP(6);
        LAS f32x4* xch = (LAS f32x4*)(smem + CV_X);
        f32x4 fin[4];
        if (half) {
#pragma unroll
            for (int i = 0; i < 4; ++i) { xch[(w * 4 + i) * 64 + lane] = acc[i]; fin[i] = acc[4 + i]; }
        } else {
#pragma unroll
            for (int i = 0; i < 4; ++i) { xch[(w * 4 + i) * 64 + lane] = acc[4 + i]; fin[i] = acc[i]; }
        }
        BAR();
#pragma unroll
        for (int i = 0; i < 4; ++i) {
            const int I = 4 * half + i, s0 = 256 * I + 16 * c + 4 * g;
            const f32x4 y = fin[i] + xch[((w ^ 1) * 4 + i) * 64 + lane];
            const v2u u = *(const LAS v2u*)(smem + cw * CV_CH + CV_U + 2 * (2044 - s0));
            const float y0 = fmaf(hi2f(u.y), dval, y[0]), y1 = fmaf(lo2f(u.y), dval, y[1]);
            const float y2 = fmaf(hi2f(u.x), dval, y[2]), y3 = fmaf(lo2f(u.x), dval, y[3]);
            const float a0 = lo2f(x0[i].x), a1 = hi2f(x0[i].x), a2 = lo2f(x0[i].y), a3 = hi2f(x0[i].y);
            const float z0 = fmaf(fmaf(a0, sa[i][0], sa[i][1]), y0, a0), z1 = fmaf(fmaf(a1, sa[i][2], sa[i][3]), y1, a1);
            const float z2 = fmaf(fmaf(a2, sb[i][0], sb[i][1]), y2, a2), z3 = fmaf(fmaf(a3, sb[i][2], sb[i][3]), y3, a3);
            const int off = (8 * I + (c >> 1)) * 32768 + ch * 64 + (((c & 1) ^ ((ch >> 2) & 1)) << 5) + 8 * g;
            ST8S((v2u{pk2bf(z0, z1), pk2bf(z2, z3)}), rws, off, OFF_X1);
            ST8S((v2u{pk2bf(y0, y1), pk2bf(y2, y3)}), rws, off, OFF_Y);
        }
    }
    STAMP(7);
    VMW(0);
    BAR();
    STAMP(8);
    if (tid < 32) __hip_atomic_fetch_add((GAS u32*)(ws + OFF_FL + FL_B + (xc * 32 + tid) * 128), 1u, RLX);
    {
        f32x4 part[3][8];
#pragma unroll
        for (int j = 0; j < 3; ++j)
#pragma unroll
            for (int i = 0; i < 8; ++i)
                part[j][i] = __builtin_bit_cast(f32x4, LD16S(rws, T0 + j < Tend ? (T0 + j) * 64 + g * 16 : OOB, OFF_CS + i * F_ * 4));
        const int wo = (cg * NP + w) * 4096 + lane * 16, rot = 2 * (w >> 1);
#pragma unroll
        for (int s = 0; s < 5; ++s)
#pragma unroll
            for (int j = 0; j < 4; ++j) W2[s][j] = LD16S(rws, wo + (j ^ rot) * 1024, OFF_W2T + s * 16384);
        SB();
#pragma unroll
        for (int j = 0; j < 3; ++j)
            *(LAS f32x4*)(smem + L_CS + (w * 3 + j) * 64 + g * 16) = ((part[j][0] + part[j][1]) + (part[j][2] + part[j][3])) + ((part[j][4] + part[j][5]) + (part[j][6] + part[j][7]));
    }
    if (w == 0) {
        const GAS u32* a = (const GAS u32*)(ws + OFF_FL + FL_B + ((lane & 7) * 32 + (b & 31)) * 128);
        u32 spins = 0;
        while (!dead && !__all(__hip_atomic_load(a, RLX) == 32u)) {
            if (++spins > SPIN_CAP) dead = true;
        }
        __builtin_amdgcn_fence(__ATOMIC_ACQUIRE, "wavefront");
        if (b == 0 && lane == 0) __hip_atomic_store((GAS u64*)(ws + OFF_FL + FL_EPOCH), tag, RLX);
    }
    BAR();
    STAMP(9);
    const u32 gvo = w * 8192 + lane * 16;
    const u32 ldsb = (u32)(size_t)smem + w * 8192;
    {
        const char* xs = ws + OFF_X1 + grp * 65536;
        glds4(xs, gvo, ldsb + L_X);
        glds4(xs + 4096, gvo, ldsb + L_X + 4096);
        glds4(xs + 32768, gvo, ldsb + L_X + 32768);
        glds4(xs + 32768 + 4096, gvo, ldsb + L_X + 32768 + 4096);
        const int wo = (cg * NP + w) * 4096 + lane * 16, rot = 2 * (w >> 1);
#pragma unroll
        for (int s = 5; s < 11; ++s)
#pragma unroll
            for (int j = 0; j < 4; ++j) W2[s][j] = LD16S(rws, s == 10 && w == 3 ? OOB : wo + (j ^ rot) * 1024, OFF_W2T + s * 16384);
        const char* ys = ws + OFF_Y + grp * 65536;
        glds4(ys, gvo, ldsb + L_Y);
        glds4(ys + 4096, gvo, ldsb + L_Y + 4096);
        glds4(ys + 32768, gvo, ldsb + L_Y + 32768);
        glds4(ys + 32768 + 4096, gvo, ldsb + L_Y + 32768 + 4096);
    }
    const char* flg = ws + OFF_FL + FL_G + grp * 1024;
    const int xrot = 16384 * (w >> 1);
#pragma unroll 1
    for (int layer = 0; layer < 2; ++layer) {
        const int hoff = OFF_H + layer * H_LAYER + 2 * grp * NP * 2048;
        int lane = tid & 63;
        PIN_V(lane);
        const int c = lane & 15, g = lane >> 4;
        const u32 gvo = w * 8192 + lane * 16;
        const LAS char* xb[2];
#pragma unroll
        for (int i = 0; i < 2; ++i) xb[i] = smem + L_X + (4 * g + (c >> 2)) * 64 + (((i ^ w ^ g) & 1) << 5) + 8 * (c & 3);
#pragma unroll
        for (int hf = 0; hf < 2; ++hf) {
            if (layer == 0) {
                if (hf == 0) VMW(48); else VMW(0);
            } else {
                if (w == 0) poll64(flg + 65536 + hf * 32768, 128, 8, tag, lane, dead);
                BAR();
                STAMP(20 + hf * 3);
                const char* xs = ws + OFF_X2 + grp * 65536 + hf * 32768;
                f32x2 pst[8];
#pragma unroll
                for (int i = 0; i < 8; ++i)
                    pst[i] = __builtin_bit_cast(f32x2, LD8S(rws, lane * 8, OFF_ST2 + (2 * grp + hf) * 4096 + i * 512));
                glds4(xs, gvo, ldsb + L_X + hf * 32768);
                glds4(xs + 4096, gvo, ldsb + L_X + hf * 32768 + 4096);
                float sm = 0.f, sq = 0.f;
#pragma unroll
                for (int i = 0; i < 8; ++i) { sm += pst[i].x; sq += pst[i].y; }
                sm = xadd32(sm);
                sq = xadd32(sq);
                const float mean = sm * (1.f / 512.f);
                const float rs = __builtin_amdgcn_rsqf(fmaxf(sq * (1.f / 512.f) - mean * mean, 0.f) + 1e-5f);
                LAS f32x2* st = (LAS f32x2*)(smem + L_MS + w * 256);
                st[lane & 31] = f32x2{rs, -mean * rs};
                const int tok0 = 16 * (((lane >> 1) ^ g) & 1) + 8 * (lane & 1);
                f32x4 ab[4];
#pragma unroll
                for (int i = 0; i < 4; ++i) ab[i] = *(LAS f32x4*)(st + tok0 + 2 * i);
                if (hf == 0) STAMP(37);
                VMW(0);
                STAMP(21 + hf * 3);
                LAS char* xp = smem + L_X + hf * 32768 + gvo;
#pragma unroll
                for (int i = 0; i < 8; ++i) {
                    const v4u xs4 = *(LAS v4u*)(xp + i * 1024), ys4 = *(LAS v4u*)(xp + L_Y + i * 1024);
                    v4u o;
#pragma unroll
                    for (int j = 0; j < 4; ++j) {
                        const float x0 = lo2f(xs4[j]), x1 = hi2f(xs4[j]);
                        o[j] = pk2bf(fmaf(fmaf(x0, ab[j][0], ab[j][1]), lo2f(ys4[j]), x0),
                                     fmaf(fmaf(x1, ab[j][2], ab[j][3]), hi2f(ys4[j]), x1));
                    }
                    *(LAS v4u*)(xp + i * 1024) = o;
                }
            }
            BAR();
            STAMP(layer == 0 ? 10 + hf * 2 : 22 + hf * 3);
#pragma unroll
            for (int kk = 0; kk < 16; ++kk)
#pragma unroll
                for (int j = 0; j < 3; ++j) { if (kk < 4) PIN_A(W1[kk][j]); else PIN_V(W1[kk][j]); }
            f32x4 acc[3][2];
#pragma unroll
            for (int j = 0; j < 3; ++j)
#pragma unroll
                for (int i = 0; i < 2; ++i) acc[j][i] = f32x4{0.f, 0.f, 0.f, 0.f};
            float sm = 0.f, sq = 0.f;
            v4u X[2][2];
#define G1_LOAD(buf, kk)                                                                                   \
    _Pragma("unroll") for (int i = 0; i < 2; ++i) {                                                        \
        const LAS char* p_ = xb[i] + ((kk) < 8 ? xrot : -xrot) + hf * 32768 + (kk) * 2048;                 \
        const v2u lo = tr_read(p_), hi = tr_read(p_ + 1024);                                               \
        X[buf][i] = v4u{lo.x, lo.y, hi.x, hi.y};                                                           \
    }
            G1_LOAD(0, 0)
#pragma unroll
            for (int kk = 0; kk < 16; ++kk) {
                SB();
                if (kk < 15) G1_LOAD((kk + 1) & 1, kk + 1)
#pragma unroll
                for (int i = 0; i < 2; ++i)
#pragma unroll
                    for (int j = 0; j < 3; ++j) acc[j][i] = mfma(W1[kk][j], X[kk & 1][i], acc[j][i]);
                if (kk < 8) {
#pragma unroll
                    for (int e = 0; e < 4; ++e) { sm = dot2(X[kk & 1][0][e], 0x3f803f80u, sm); sq = dot2(X[kk & 1][0][e], X[kk & 1][0][e], sq); }
                }
            }
            SB();
            if (layer == 0 && hf == 1) STAMP(35);
            sm = xadd32(xadd16(sm));
            sq = xadd32(xadd16(sq));
            LAS f32x2* ls = (LAS f32x2*)(smem + L_ST + hf * 512);
            if (g == 0) ls[w * 16 + c] = f32x2{sm, sq};
            BAR();
            if (layer == 0 && hf == 1) STAMP(36);
#pragma unroll
            for (int i = 0; i < 2; ++i) {
                const int tt = (i ^ w) & 1;
                const f32x2 s2 = ls[tt * 16 + c] + ls[(tt + 2) * 16 + c];
                const float mean = s2.x * (1.f / 512.f);
                const float rs = __builtin_amdgcn_rsqf(fmaxf(s2.y * (1.f / 512.f) - mean * mean, 0.f) + 1e-5f);
                const float nb = -mean * rs;
#pragma unroll
                for (int j = 0; j < 3; ++j) {
                    const int T = T0 + j;
                    const f32x4 u = acc[j][i] * rs + *(const LAS f32x4*)(smem + L_CS + (w * 3 + j) * 64 + g * 16) * nb;
                    const s16x2 z = {0, 0};
                    const v2u o = {__builtin_bit_cast(u32, __builtin_elementwise_max(__builtin_bit_cast(s16x2, pk2bf(u[0], u[1])), z)),
                                   __builtin_bit_cast(u32, __builtin_elementwise_max(__builtin_bit_cast(s16x2, pk2bf(u[2], u[3])), z))};
                    ST8S(o, rws, T < Tend ? ((T >> 1) * 2 + tt) * 1024 + lane * 16 + 8 * (T & 1) : OOB, hoff + hf * NP * 2048);
                }
            }
            STAMP(layer == 0 ? 11 + hf * 2 : 26 + hf);
            VMW(0);
            BAR();
            if (tid == 0) __hip_atomic_store((GAS u64*)(flg + layer * 131072 + hf * 32768 + cg * 128), tag, RLX);
        }
#pragma unroll
        for (int hf = 0; hf < 2; ++hf) {
            if (w == 0) poll64(flg + layer * 131072 + hf * 32768, 128, 8, tag, lane, dead);
            BAR();
            STAMP(layer == 0 ? 14 + hf * 3 : 28 + hf * 3);
            v4u H[8][2];
#define H_LOAD(s)                                                                                                       \
    _Pragma("unroll") for (int i = 0; i < 2; ++i)                                                                       \
        H[(s) & 7][i] = LD16S(rws, (s) == 10 && w == 3 ? OOB : (w * 2 + ((i ^ w) & 1)) * 1024 + lane * 16, hoff + hf * NP * 2048 + (s) * 8192);
#pragma unroll
            for (int s = 0; s < 8; ++s) H_LOAD(s)
            SB();
#pragma unroll
            for (int s = 0; s < 11; ++s)
#pragma unroll
                for (int j = 0; j < 4; ++j) PIN_A(W2[s][j]);
            f32x4 acc[2][4];
#pragma unroll
            for (int i = 0; i < 2; ++i)
#pragma unroll
                for (int j = 0; j < 4; ++j) acc[i][j] = f32x4{0.f, 0.f, 0.f, 0.f};
#pragma unroll
            for (int s = 0; s < 11; ++s) {
#pragma unroll
                for (int i = 0; i < 2; ++i)
#pragma unroll
                    for (int j = 0; j < 4; ++j) acc[i][j] = mfma(H[s & 7][i], W2[s][j], acc[i][j]);
                SB();
                if (s < 3) H_LOAD(s + 8)
                SB();
            }
            STAMP(layer == 0 ? 15 + hf * 3 : 29 + hf * 3);
            LAS f32x4* red = (LAS f32x4*)(smem + L_RED);
#pragma unroll
            for (int i = 0; i < 2; ++i)
#pragma unroll
                for (int j = 0; j < 4; ++j) {
                    const int src = i + 2 * (j >> 1);
                    if (src) red[(((w ^ src) * 3 + src - 1) * 2 + (j & 1)) * 64 + lane] = acc[i][j];
                }
            BAR();
            const int tt = w & 1, chl = 32 * (w >> 1) + 2 * c;
            float xf[2][4];
#pragma unroll
            for (int j = 0; j < 2; ++j) {
                const int ch = 64 * cg + chl + j;
                const v2u res = *(const LAS v2u*)(smem + L_X + hf * 32768 + ch * 64 + ((tt ^ ((ch >> 2) & 1)) << 5) + 8 * g);
                const f32x4 t = (acc[0][j] + red[((w * 3 + 0) * 2 + j) * 64 + lane]) +
                                (red[((w * 3 + 1) * 2 + j) * 64 + lane] + red[((w * 3 + 2) * 2 + j) * 64 + lane]);
                xf[j][0] = lo2f(res.x) + t[0];
                xf[j][1] = hi2f(res.x) + t[1];
                xf[j][2] = lo2f(res.y) + t[2];
                xf[j][3] = hi2f(res.y) + t[3];
            }
            if (layer == 1) {
#pragma unroll
                for (int r = 0; r < 4; ++r)
                    __builtin_amdgcn_raw_buffer_store_b32(pk2bf(xf[0][r], xf[1][r]), mkrs(out, 2 * MB), (16 * tt + 4 * g + r) * 1024 + chl * 2,
                                                          (64 * grp + 32 * hf) * 1024 + cg * 128, 0);
            } else {
                float sm[4], sq[4];
                u32 xo[2][2];
#pragma unroll
                for (int j = 0; j < 2; ++j) {
                    const int ch = 64 * cg + chl + j;
                    xo[j][0] = pk2bf(xf[j][0], xf[j][1]);
                    xo[j][1] = pk2bf(xf[j][2], xf[j][3]);
                    ST8S((v2u{xo[j][0], xo[j][1]}), rws, ch * 64 + ((tt ^ ((ch >> 2) & 1)) << 5) + 8 * g, OFF_X2 + (2 * grp + hf) * 32768);
                }
#pragma unroll
                for (int r = 0; r < 4; ++r) {
                    const float x0 = (r & 1) ? hi2f(xo[0][r >> 1]) : lo2f(xo[0][r >> 1]);
                    const float x1 = (r & 1) ? hi2f(xo[1][r >> 1]) : lo2f(xo[1][r >> 1]);
                    sm[r] = row16sum(x0 + x1);
                    sq[r] = row16sum(fmaf(x0, x0, x1 * x1));
                }
                const f32x4 s4 = (c & 1) ? f32x4{sm[2], sq[2], sm[3], sq[3]} : f32x4{sm[0], sq[0], sm[1], sq[1]};
                ST16S(__builtin_bit_cast(v4u, s4), rws, c < 2 ? (cg * 2 + (w >> 1)) * 256 + (16 * tt + 4 * g + 2 * c) * 8 : OOB,
                      OFF_ST2 + (2 * grp + hf) * 4096);
            }
            STAMP(layer == 0 ? 16 + hf * 3 : 30 + hf * 3);
            if (layer == 0) {
                VMW(0);
                BAR();
                if (tid == 0) __hip_atomic_store((GAS u64*)(flg + 65536 + hf * 32768 + cg * 128), tag, RLX);
            } else {
                BAR();
            }
        }
    }
}

extern "C" void kernel_launch(void* const* d_in, const int* in_sizes, int n_in, void* d_out, int out_size, void* d_ws,
                              size_t ws_size, hipStream_t stream) {
    constexpr int lds = L_BYTES;
    (void)hipFuncSetAttribute((const void*)k_all, hipFuncAttributeMaxDynamicSharedMemorySize, lds);
    k_all<<<256, 256, lds, stream>>>((const u16*)d_in[0], (const u16*)d_in[1], (const u16*)d_in[2], (const u16*)d_in[3],
                                     (const u16*)d_in[4], (const u16*)d_in[5], (const u16*)d_in[7], (const u16*)d_in[8],
                                     (u16*)d_out, (char*)d_ws);
}
```

```cpp
#include <hip/hip_runtime.h>
#include <hip/hip_bf16.h>
#include <stdint.h>

typedef unsigned short u16;
typedef unsigned int u32;
typedef unsigned long long u64;
typedef u32 v4u __attribute__((ext_vector_type(4)));
typedef u32 v2u __attribute__((ext_vector_type(2)));
typedef float f32x4 __attribute__((ext_vector_type(4)));
typedef float f32x2 __attribute__((ext_vector_type(2)));
typedef short bf16x8 __attribute__((ext_vector_type(8)));
typedef short s16x4 __attribute__((ext_vector_type(4)));
typedef short s16x2 __attribute__((ext_vector_type(2)));
typedef __bf16 bf16x2 __attribute__((ext_vector_type(2)));
#define LAS __attribute__((address_space(3)))
#define GAS __attribute__((address_space(1)))
#define DEV __device__ __forceinline__
#define RLX __ATOMIC_RELAXED, __HIP_MEMORY_SCOPE_AGENT
typedef __amdgpu_buffer_rsrc_t rsrc_t;

constexpr int S_ = 2048, D_ = 512, F_ = 1376;
constexpr int NP = 43;
constexpr int NT = 86;
constexpr int MB = 1 << 20;
constexpr int OFF_FL = 0, OFF_ST0 = 512 * 1024, OFF_ST2 = 640 * 1024;
constexpr int OFF_UR = 1 * MB, OFF_X0T = 3 * MB, OFF_Y = 5 * MB, OFF_X1 = 7 * MB, OFF_X2 = 9 * MB, OFF_W1T = 11 * MB,
              OFF_W2T = 13 * MB, OFF_H = 15 * MB, H_LAYER = 6 * MB, WS_USED = 27 * MB;
constexpr int FL_EPOCH = 0, FL_A2 = 1024, FL_A = 4096, FL_B = 40960, FL_G = 81920;
constexpr int SC1 = 16, OOB = 0x40000000;
constexpr int L_X = 0, L_Y = 65536, L_RED = 131072, L_ST = 155648, L_MS = L_ST + 1024, L_CS = L_MS + 1024, L_BYTES = L_CS + 1024;
constexpr int CV_E = 0, CV_O = 4160, CV_U = 8320, CV_CH = 12928, CV_X = 2 * CV_CH, L_P0 = 49152;
constexpr u32 SPIN_CAP = 1u << 18;
#define NA1 9
#define NW_CONV 18
#define NW2_PRE 4

DEV float lo2f(u32 d) { return __builtin_bit_cast(float, d << 16); }
DEV float hi2f(u32 d) { return __builtin_bit_cast(float, d & 0xffff0000u); }
DEV f32x2 up2(u32 d) { return f32x2{lo2f(d), hi2f(d)}; }
DEV u32 pk2(f32x2 x) { return __builtin_bit_cast(u32, __builtin_convertvector(x, bf16x2)); }
DEV u32 pk2bf(float a, float b) { return pk2(f32x2{a, b}); }
DEV f32x2 fma2(f32x2 a, f32x2 b, f32x2 c) { return __builtin_elementwise_fma(a, b, c); }
DEV f32x4 mfma(v4u a, v4u b, f32x4 c) {
    return __builtin_amdgcn_mfma_f32_16x16x32_bf16(__builtin_bit_cast(bf16x8, a), __builtin_bit_cast(bf16x8, b), c, 0, 0, 0);
}
DEV v2u tr_read(const LAS char* p) {
    return __builtin_bit_cast(v2u, __builtin_amdgcn_ds_read_tr16_b64_v4i16((LAS s16x4*)p));
}
DEV float xadd16(float v) {
    auto r = __builtin_amdgcn_permlane16_swap(__builtin_bit_cast(u32, v), __builtin_bit_cast(u32, v), false, false);
    const u32 r0 = r[0], r1 = r[1];
    return __builtin_bit_cast(float, r0) + __builtin_bit_cast(float, r1);
}
DEV float xadd32(float v) {
    auto r = __builtin_amdgcn_permlane32_swap(__builtin_bit_cast(u32, v), __builtin_bit_cast(u32, v), false, false);
    const u32 r0 = r[0], r1 = r[1];
    return __builtin_bit_cast(float, r0) + __builtin_bit_cast(float, r1);
}
template <int CTRL> DEV float dppadd(float v) {
    return v + __builtin_bit_cast(float, __builtin_amdgcn_update_dpp(0, __builtin_bit_cast(int, v), CTRL, 0xf, 0xf, true));
}
DEV float row16sum(float v) {
    v = dppadd<0x121>(v);
    v = dppadd<0x122>(v);
    v = dppadd<0x124>(v);
    v = dppadd<0x128>(v);
    return v;
}
DEV rsrc_t mkrs(const void* p, int bytes) { return __builtin_amdgcn_make_buffer_rsrc((void*)p, 0, bytes, 0x00020000); }
#define LD16(r, vo, so) __builtin_amdgcn_raw_buffer_load_b128(r, vo, so, 0)
#define LD4(r, vo, so) __builtin_amdgcn_raw_buffer_load_b32(r, vo, so, 0)
#define LD16S(r, vo, so) __builtin_amdgcn_raw_buffer_load_b128(r, vo, so, SC1)
#define LD8S(r, vo, so) __builtin_amdgcn_raw_buffer_load_b64(r, vo, so, SC1)
#define ST16S(v, r, vo, so) __builtin_amdgcn_raw_buffer_store_b128(v, r, vo, so, SC1)
#define ST8S(v, r, vo, so) __builtin_amdgcn_raw_buffer_store_b64(v, r, vo, so, SC1)
#define SB() __builtin_amdgcn_sched_barrier(0)
#define PIN_A(x) asm volatile("" : "+a"(x))
#define PIN_V(x) asm volatile("" : "+v"(x))
#define BAR() do { SB(); asm volatile("s_waitcnt lgkmcnt(0)\n\ts_barrier" ::: "memory"); SB(); } while (0)
#define VMW(n) do { SB(); asm volatile("s_waitcnt vmcnt(%0)" ::"n"(n) : "memory"); SB(); } while (0)
DEV void glds8(const char* sbase, u32 voff, u32 lds) {
    u32 keep;
    asm volatile(
        "s_mov_b32 %0, m0\n\ts_mov_b32 m0, %3\n\ts_nop 0\n\t"
        "global_load_lds_dwordx4 %1, %2 offset:-4096 sc1\n\t"
        "global_load_lds_dwordx4 %1, %2 offset:-3072 sc1\n\t"
        "global_load_lds_dwordx4 %1, %2 offset:-2048 sc1\n\t"
        "global_load_lds_dwordx4 %1, %2 offset:-1024 sc1\n\t"
        "global_load_lds_dwordx4 %1, %2 sc1\n\t"
        "global_load_lds_dwordx4 %1, %2 offset:1024 sc1\n\t"
        "global_load_lds_dwordx4 %1, %2 offset:2048 sc1\n\t"
        "global_load_lds_dwordx4 %1, %2 offset:3072 sc1\n\t"
        "s_mov_b32 m0, %0"
        : "=&s"(keep) : "v"(voff), "s"(sbase + 4096), "s"(lds + 4096) : "memory");
}
DEV void glds4(const char* sbase, u32 voff, u32 lds) {
    u32 keep;
    asm volatile(
        "s_mov_b32 %0, m0\n\ts_mov_b32 m0, %3\n\ts_nop 0\n\t"
        "global_load_lds_dwordx4 %1, %2 sc1\n\t"
        "global_load_lds_dwordx4 %1, %2 offset:1024 sc1\n\t"
        "global_load_lds_dwordx4 %1, %2 offset:2048 sc1\n\t"
        "global_load_lds_dwordx4 %1, %2 offset:3072 sc1\n\t"
        "s_mov_b32 m0, %0"
        : "=&s"(keep) : "v"(voff), "s"(sbase), "s"(lds) : "memory");
}
DEV bool tagok(v2u f, u64 tag) { return __all(f.x == (u32)tag && f.y == (u32)(tag >> 32)); }
#define POLL_FINISH(f, vo, so)                                            \
    do {                                                                  \
        u32 spins_ = 0;                                                   \
        while (!tagok(f, tag)) {                                          \
            if (dead || ++spins_ > SPIN_CAP) { dead = true; break; }      \
            asm volatile("" ::: "memory");                                \
            f = LD8S(rws, vo, so);                                        \
        }                                                                 \
        __builtin_amdgcn_fence(__ATOMIC_ACQUIRE, "wavefront");            \
        SB();                                                             \
    } while (0)

#define STAMP(i)

__global__ __launch_bounds__(256, 1) void k_all(const u16* __restrict__ q, const u16* __restrict__ k, const u16* __restrict__ v,
                                                const u16* __restrict__ sk, const u16* __restrict__ ssmk,
                                                const u16* __restrict__ Dv, const u16* __restrict__ w1,
                                                const u16* __restrict__ w2, u16* __restrict__ out, char* __restrict__ ws) {
    extern __shared__ __attribute__((aligned(16))) char smem_[];
    LAS char* smem = (LAS char*)smem_;
    const int b = blockIdx.x, tid = threadIdx.x, w = __builtin_amdgcn_readfirstlane(tid >> 6);
    const int xc = b & 7, jb = b >> 3;
    const rsrc_t rws = mkrs(ws, WS_USED);
    bool dead = false;
    STAMP(0);
    const int ch0 = 64 * xc + 2 * jb;
    const int grp = 4 * xc + (b >> 6), cg = jb & 7;
    const int T0 = 11 * cg + 3 * w, Tend = cg == 7 ? NT : 11 * cg + 11;
    u64 tag;
    v4u W1[16][3], W2[11][4];
    {
        const int lane = tid & 63, c = lane & 15, g = lane >> 4;
        {
            const int row = 2 * (tid >> 3), c8 = tid & 7, s0 = 64 * jb + row;
            const int vo = s0 * 1024 + xc * 128 + c8 * 16;
            const rsrc_t rk = mkrs(k, 2 * MB), rv = mkrs(v, 2 * MB), rq = mkrs(q, 2 * MB), rh = mkrs(ssmk + ch0 * S_, 8192);
            v4u kk[5], vv[2], qq[2], sw[4], xr[2], hh[2];
            u32 hn[2], wd[16];
#pragma unroll
            for (int t = 0; t < 5; ++t) kk[t] = LD16(rk, s0 + t - 3 >= 0 ? vo + (t - 3) * 1024 : OOB, 0);
#pragma unroll
            for (int t = 0; t < 2; ++t) vv[t] = LD16(rv, vo + t * 1024, 0);
#pragma unroll
            for (int t = 0; t < 4; ++t) sw[t] = *(const v4u*)(sk + (64 * xc + 8 * c8) * 4 + 8 * t);
            const u64 epoch = __hip_atomic_load((const GAS u64*)(ws + OFF_FL + FL_EPOCH), RLX);
#pragma unroll
            for (int i = 0; i < 2; ++i) {
                hh[i] = LD16(rh, tid * 16, i * 4096);
                hn[i] = LD4(rh, tid < 255 ? tid * 16 + 16 : OOB, i * 4096);
            }
            const bool is1 = b < 2 * NP;
            const int p = is1 ? b >> 1 : (b - 2 * NP) >> 1;
            const rsrc_t rw = mkrs(is1 ? w1 : w2, b < 4 * NP ? F_ * D_ * 2 : 0);
            const int wvo = is1 ? ((32 * (8 * (b & 1) + 2 * w) + 4 * g) * F_ + 32 * p + 2 * c) * 2
                                : ((32 * p + 8 * g) * D_ + 64 * (4 * (b & 1) + w) + 2 * c) * 2;
            const int s3 = is1 ? 32 * F_ * 2 : 64, s2 = is1 ? 16 * F_ * 2 : D_ * 2, s1 = is1 ? F_ * 2 : 2 * D_ * 2;
#define P0_SECONDARY()                                                                                     \
    _Pragma("unroll") for (int t = 0; t < 2; ++t) qq[t] = LD16(rq, vo + t * 1024, 0);                      \
    _Pragma("unroll") for (int i = 0; i < 2; ++i) xr[i] = LD16(rq, (8 * b + 2 * w + i) * 1024 + lane * 16, 0); \
    _Pragma("unroll") for (int e = 0; e < 16; ++e) wd[e] = LD4(rw, wvo, (e >> 3) * s3 + ((e >> 2) & 1) * s2 + (e & 3) * s1);
            SB();
#pragma unroll
            for (int t = 0; t < 2; ++t) {
                v4u o;
#pragma unroll
                for (int j = 0; j < 4; ++j) {
                    f32x2 a = up2(kk[t][j]) * f32x2{hi2f(sw[j].y), hi2f(sw[j].w)};
                    a = fma2(up2(kk[t + 1][j]), f32x2{lo2f(sw[j].y), lo2f(sw[j].w)}, a);
                    a = fma2(up2(kk[t + 2][j]), f32x2{hi2f(sw[j].x), hi2f(sw[j].z)}, a);
                    a = fma2(up2(kk[t + 3][j]), f32x2{lo2f(sw[j].x), lo2f(sw[j].z)}, a);
                    o[j] = pk2(a * up2(vv[t][j]));
                }
                *(LAS v4u*)(smem + L_P0 + (row + t) * 128 + c8 * 16) = o;
            }
            BAR();
            STAMP(1);
            const LAS char* base = smem + L_P0 + 32 * w + 8 * (c & 3);
            const int qd = c >> 2, dst = (64 * xc + 16 * w + c) * 4096 + 16 * g;
            {
                const v2u a0 = tr_read(base + (63 - 8 * g - qd) * 128), a1 = tr_read(base + (59 - 8 * g - qd) * 128);
                const v2u a2 = tr_read(base + (31 - 8 * g - qd) * 128), a3 = tr_read(base + (27 - 8 * g - qd) * 128);
                ST16S((v4u{a0.x, a0.y, a1.x, a1.y}), rws, dst, OFF_UR + (1984 - 64 * jb) * 2);
                ST16S((v4u{a2.x, a2.y, a3.x, a3.y}), rws, dst + 64, OFF_UR + (1984 - 64 * jb) * 2);
            }
            SB();
            P0_SECONDARY()
            SB();
            tag = epoch * 6364136223846793005ull + 1442695040888963407ull;
            const v4u zero = {0, 0, 0, 0};
            if (tid < 60) {
                const int i = tid >= 30, r = tid - 30 * i;
                *(LAS v4u*)(smem + i * CV_CH + CV_U + 4096 + r * 16) = zero;
            } else if (tid >= 64 && tid < 70) {
                const int r = tid - 64, i = r >= 3, m = r - 3 * i;
                *(LAS v4u*)(smem + i * CV_CH + (m < 2 ? CV_E + 16 * m : CV_O)) = zero;
            }
#pragma unroll
            for (int i = 0; i < 2; ++i) {
                *(LAS v4u*)(smem + i * CV_CH + CV_E + 32 + tid * 16) = hh[i];
                v4u o;
                o.x = __builtin_amdgcn_alignbyte(hh[i].y, hh[i].x, 2);
                o.y = __builtin_amdgcn_alignbyte(hh[i].z, hh[i].y, 2);
                o.z = __builtin_amdgcn_alignbyte(hh[i].w, hh[i].z, 2);
                o.w = __builtin_amdgcn_alignbyte(hn[i], hh[i].w, 2);
                *(LAS v4u*)(smem + i * CV_CH + CV_O + 32 + tid * 16) = o;
                if (tid == 0) *(LAS v4u*)(smem + i * CV_CH + CV_O + 16) = v4u{0, 0, 0, hh[i].x << 16};
            }
            VMW(20);
            BAR();
            STAMP(2);
            if (tid == 0) __hip_atomic_store((GAS u64*)(ws + OFF_FL + FL_A + (xc * 32 + jb) * 128), tag, RLX);
            if (tid == 64) __hip_atomic_store((GAS u32*)(ws + OFF_FL + FL_B + b * 128), 0u, RLX);
#pragma unroll
            for (int t = 0; t < 2; ++t) *(LAS v4u*)(smem + L_P0 + 8192 + (row + t) * 128 + c8 * 16) = qq[t];
            f32x2 sta, stb;
#pragma unroll
            for (int i = 0; i < 2; ++i) {
                f32x2 x[4];
#pragma unroll
                for (int j = 0; j < 4; ++j) x[j] = up2(xr[i][j]);
                const f32x2 s2v = (x[0] + x[1]) + (x[2] + x[3]);
                const float mean = xadd32(xadd16(row16sum(s2v.x + s2v.y))) * (1.f / 512.f);
                f32x2 sq = {0.f, 0.f};
#pragma unroll
                for (int j = 0; j < 4; ++j) sq = fma2(x[j] - mean, x[j] - mean, sq);
                const float rs = __builtin_amdgcn_rsqf(xadd32(xadd16(row16sum(sq.x + sq.y))) * (1.f / 512.f) + 1e-5f);
                sta[i] = rs;
                stb[i] = -mean * rs;
            }
            ST8S(__builtin_bit_cast(v2u, lane ? stb : sta), rws, lane < 2 ? (8 * b + 2 * w) * 4 + lane * 8192 : OOB, OFF_ST0);
            BAR();
            {
                const v2u a0 = tr_read(base + 8192 + (8 * g + qd) * 128), a1 = tr_read(base + 8192 + (8 * g + 4 + qd) * 128);
                const v2u a2 = tr_read(base + 8192 + (32 + 8 * g + qd) * 128), a3 = tr_read(base + 8192 + (36 + 8 * g + qd) * 128);
                ST16S((v4u{a0.x, a0.y, a1.x, a1.y}), rws, dst, OFF_X0T + 128 * jb);
                ST16S((v4u{a2.x, a2.y, a3.x, a3.y}), rws, dst + 64, OFF_X0T + 128 * jb);
            }
#pragma unroll
            for (int u = 0; u < 4; ++u) {
                v4u fr;
#pragma unroll
                for (int i = 0; i < 4; ++i)
                    fr[i] = __builtin_amdgcn_perm(wd[8 * (u >> 1) + 2 * i + 1], wd[8 * (u >> 1) + 2 * i], (u & 1) ? 0x07060302u : 0x05040100u);
                const int T = 2 * p + (u & 1), tc = T / 11, tr = T - 11 * tc, tw = tr / 3, tj = tr - 3 * tw;
                const int kq = 8 * (b & 1) + 2 * w + (u >> 1);
                const int o1 = OFF_W1T + (tc * 4 + tw) * 49152 + (((kq - 8 * (tw >> 1)) & 15) * 3 + tj) * 1024;
                const int o2 = OFF_W2T + ((4 * (b & 1) + w) * NP + p) * 4096 + u * 1024;
                ST16S(fr, rws, b < 4 * NP ? lane * 16 : OOB, is1 ? o1 : o2);
            }
        }
        STAMP(3);
        VMW(0);
        BAR();
        STAMP(4);
        if (tid == 64) __hip_atomic_store((GAS u64*)(ws + OFF_FL + FL_A2 + b * 8), tag, RLX);
        if (w == 0) {
            v2u f = LD8S(rws, (lane & 31) * 128, OFF_FL + FL_A + xc * 4096);
            POLL_FINISH(f, (lane & 31) * 128, OFF_FL + FL_A + xc * 4096);
        }
        BAR();
        STAMP(5);
        const int cw = w >> 1, half = w & 1, ch = ch0 + cw;
        {
            u32 keep;
            asm volatile(
                "s_mov_b32 %0, m0\n\ts_mov_b32 m0, %3\n\ts_nop 0\n\t"
                "global_load_lds_dwordx4 %1, %2 sc1\n\t"
                "global_load_lds_dwordx4 %1, %2 offset:1024 sc1\n\t"
                "s_mov_b32 m0, %0"
                : "=&s"(keep) : "v"(lane * 16), "s"(ws + OFF_UR + ch * 4096 + half * 2048),
                  "s"((u32)(size_t)(smem + cw * CV_CH + CV_U + half * 2048)) : "memory");
        }
        v4u fa0 = LD16S(rws, lane * 32, OFF_FL + FL_A2), fa1 = LD16S(rws, lane * 32 + 16, OFF_FL + FL_A2);
        const u32 draw = __builtin_amdgcn_raw_buffer_load_b16(mkrs(Dv, 1024), 0, ch * 2, 0);
        const LAS char* ab = smem + cw * CV_CH + ((c & 1) ? CV_E + 2 * (1 + c) : CV_O + 2 * c) + 16 * g + 256 * half;
        const LAS char* bb = smem + cw * CV_CH + CV_U + 4064 - 3584 - 32 * c + 16 * g + 256 * half;
        v4u A[2][8], B[2][8];
#define CV_LA(buf, r, i) { const LAS u32* pa = (const LAS u32*)(ab + 512 * (i) + 64 * (r)); A[buf][i] = v4u{pa[0], pa[1], pa[2], pa[3]}; }
#define CV_LB(buf, r, i) B[buf][i] = *(const LAS v4u*)(bb + 512 * (7 - (i)) + 64 * (r));
#pragma unroll
        for (int i = 0; i < 8; ++i) CV_LA(0, 0, i)
        VMW(3);
        BAR();
        STAMP(6);
#pragma unroll
        for (int i = 0; i < 8; ++i) CV_LB(0, 0, i)
        f32x4 acc[8];
#pragma unroll
        for (int i = 0; i < 8; ++i) acc[i] = f32x4{0.f, 0.f, 0.f, 0.f};
        v2u x0[4], uu[4];
        f32x4 sa[4], sb[4];
        const int w1o = OFF_W1T + (cg * 4 + w) * 49152;
        int w1v[3];
#pragma unroll
        for (int j = 0; j < 3; ++j) w1v[j] = T0 + j < Tend ? j * 1024 + lane * 16 : OOB;
        const int tk = 1024 * half + 16 * c + 4 * g;
#define W1_LOAD(n) W1[(n) / 3][(n) % 3] = LD16S(rws, w1v[(n) % 3], w1o + ((n) / 3) * 3072)
#pragma unroll
        for (int r = 0; r < 4; ++r) {
            int idx = 0;
#pragma unroll
            for (int al = 0; al < 8; ++al)
#pragma unroll
                for (int be = 0; be + al < 8; ++be) {
                    acc[al + be] = mfma(A[r & 1][al], B[r & 1][be], acc[al + be]);
                    if (r < 3 && idx >= 4 && idx < 20) {
                        if (idx & 1) CV_LA((r + 1) & 1, r + 1, (idx - 4) >> 1) else CV_LB((r + 1) & 1, r + 1, (idx - 4) >> 1)
                    }
                    if (r == 3 && idx < 4) uu[idx] = *(const LAS v2u*)(smem + cw * CV_CH + CV_U + 2 * (2044 - tk) - 512 * idx);
                    if (r >= 1 && idx % 3 == 2) {
                        const int n = (r - 1) * 12 + idx / 3;
                        if (n < 4) x0[n] = LD8S(rws, tk * 2 + n * 512, OFF_X0T + ch * 4096);
                        else if (n < 8) sa[n - 4] = __builtin_bit_cast(f32x4, LD16S(rws, tk * 4 + (n - 4) * 1024, OFF_ST0));
                        else if (n < 12) sb[n - 8] = __builtin_bit_cast(f32x4, LD16S(rws, tk * 4 + (n - 8) * 1024, OFF_ST0 + 8192));
                        else if (n - 12 < NW_CONV) W1_LOAD(n - 12);
                    }
                    SB();
                    ++idx;
                }
            if (r == 0) {
                const u32 tl = (u32)tag, th = (u32)(tag >> 32);
                u32 spins = 0;
                while (!__all(fa0.x == tl && fa0.z == tl && fa1.x == tl && fa1.z == tl && fa0.y == th && fa0.w == th && fa1.y == th && fa1.w == th)) {
                    if (dead || ++spins > SPIN_CAP) { dead = true; break; }
                    asm volatile("" ::: "memory");
                    fa0 = LD16S(rws, lane * 32, OFF_FL + FL_A2);
                    fa1 = LD16S(rws, lane * 32 + 16, OFF_FL + FL_A2);
                }
                __builtin_amdgcn_fence(__ATOMIC_ACQUIRE, "wavefront");
                SB();
                STAMP(7);
            }
        }
        STAMP(8);
        LAS f32x4* xch = (LAS f32x4*)(smem + CV_X);
        f32x4 fin[4];
        if (half) {
#pragma unroll
            for (int i = 0; i < 4; ++i) { xch[(w * 4 + i) * 64 + lane] = acc[i]; fin[i] = acc[4 + i]; }
        } else {
#pragma unroll
            for (int i = 0; i < 4; ++i) { xch[(w * 4 + i) * 64 + lane] = acc[4 + i]; fin[i] = acc[i]; }
        }
        BAR();
        {
            f32x4 oth[4];
#pragma unroll
            for (int i = 0; i < 4; ++i) oth[i] = xch[((w ^ 1) * 4 + i) * 64 + lane];
            SB();
            const float dval = lo2f(draw);
            const int off = (32 * half + (c >> 1)) * 32768 + ch * 64 + (((c & 1) ^ ((ch >> 2) & 1)) << 5) + 8 * g;
#pragma unroll
            for (int i = 0; i < 4; ++i) {
                const f32x4 y = fin[i] + oth[i];
                const f32x2 ya = fma2(f32x2{hi2f(uu[i].y), lo2f(uu[i].y)}, f32x2{dval, dval}, y.xy);
                const f32x2 yb = fma2(f32x2{hi2f(uu[i].x), lo2f(uu[i].x)}, f32x2{dval, dval}, y.zw);
                const f32x2 xa = up2(x0[i].x), xb2 = up2(x0[i].y);
                const f32x2 za = fma2(fma2(xa, sa[i].xy, sb[i].xy), ya, xa), zb = fma2(fma2(xb2, sa[i].zw, sb[i].zw), yb, xb2);
                ST8S((v2u{pk2(za), pk2(zb)}), rws, off, OFF_X1 + i * 8 * 32768);
                ST8S((v2u{pk2(ya), pk2(yb)}), rws, off, OFF_Y + i * 8 * 32768);
                SB();
            }
        }
        STAMP(9);
#pragma unroll
        for (int n = NW_CONV; n < NW_CONV + 8; ++n) W1_LOAD(n);
        VMW(8);
        BAR();
        STAMP(10);
        if (tid < 32) __hip_atomic_fetch_add((GAS u32*)(ws + OFF_FL + FL_B + (xc * 32 + tid) * 128), 1u, RLX);
#pragma unroll
        for (int n = NW_CONV + 8; n < 48; ++n) W1_LOAD(n);
        const int w2o = OFF_W2T + (cg * NP + w) * 4096, w2v = lane * 16 + 2048 * (w >> 1);
#define W2_LOAD(s, j) W2[s][j] = LD16S(rws, (s) == 10 && w == 3 ? OOB : (w2v ^ (((j) >> 1) * 2048)) + ((j) & 1) * 1024, w2o + (s) * 16384)
#pragma unroll
        for (int s = 0; s < NW2_PRE; ++s)
#pragma unroll
            for (int j = 0; j < 4; ++j) W2_LOAD(s, j);
        SB();
        {
            const v4u ones = {0x3f803f80u, 0x3f803f80u, 0x3f803f80u, 0x3f803f80u};
            f32x4 cs[3];
#pragma unroll
            for (int j = 0; j < 3; ++j) cs[j] = f32x4{0.f, 0.f, 0.f, 0.f};
#pragma unroll
            for (int kk = 0; kk < 16; ++kk)
#pragma unroll
                for (int j = 0; j < 3; ++j) {
                    if (kk * 3 + j < NA1) PIN_A(W1[kk][j]); else PIN_V(W1[kk][j]);
                    cs[j] = mfma(W1[kk][j], ones, cs[j]);
                }
#pragma unroll
            for (int j = 0; j < 3; ++j) *(LAS f32x4*)(smem + L_CS + (w * 3 + j) * 64 + g * 16) = cs[j];
        }
        STAMP(11);
        if (w == 0) {
            const GAS u32* a = (const GAS u32*)(ws + OFF_FL + FL_B + ((lane & 7) * 32 + (b & 31)) * 128);
            u32 spins = 0;
            while (!dead && !__all(__hip_atomic_load(a, RLX) == 32u)) {
                if (++spins > SPIN_CAP) dead = true;
            }
            __builtin_amdgcn_fence(__ATOMIC_ACQUIRE, "wavefront");
            if (b == 0 && lane == 0) __hip_atomic_store((GAS u64*)(ws + OFF_FL + FL_EPOCH), tag, RLX);
        }
        BAR();
        STAMP(12);
        const u32 ldsb = (u32)(size_t)smem + w * 8192;
        glds8(ws + OFF_X1 + grp * 65536 + w * 8192, lane * 16, ldsb + L_X);
        glds8(ws + OFF_X1 + grp * 65536 + 32768 + w * 8192, lane * 16, ldsb + L_X + 32768);
#pragma unroll
        for (int s = NW2_PRE; s < 11; ++s)
#pragma unroll
            for (int j = 0; j < 4; ++j) W2_LOAD(s, j);
        VMW(8 + 4 * (11 - NW2_PRE));
    }
#pragma unroll 1
    for (int layer = 0; layer < 2; ++layer) {
        int bl = blockIdx.x, w = __builtin_amdgcn_readfirstlane(tid >> 6);
        asm volatile("" : "+s"(bl), "+s"(w));
        const int grp = 4 * (bl & 7) + (bl >> 6), cg = (bl >> 3) & 7;
        const int T0 = 11 * cg + 3 * w, Tend = cg == 7 ? NT : 11 * cg + 11;
        const int fgo = OFF_FL + FL_G + grp * 1024;
        const u32 ldsb = (u32)(size_t)smem + w * 8192;
        const int xrot = 16384 * (w >> 1);
        const int hoff = OFF_H + layer * H_LAYER + 2 * grp * NP * 2048;
        int lane = tid & 63;
        PIN_V(lane);
        const int c = lane & 15, g = lane >> 4;
        const int fvo = (lane & 7) * 128;
        v2u fl;
        v4u H[8][2];
        const int hv0 = (w * 2 + (w & 1)) * 1024 + lane * 16, hv1 = hv0 ^ 1024;
#define H_LOAD(hf, s)                                                                       \
    {                                                                                       \
        H[(s) & 7][0] = LD16S(rws, (s) == 10 && w == 3 ? OOB : hv0, hoff + (hf) * NP * 2048 + (s) * 8192); \
        H[(s) & 7][1] = LD16S(rws, (s) == 10 && w == 3 ? OOB : hv1, hoff + (hf) * NP * 2048 + (s) * 8192); \
    }
#define X2_REQUEST(hf)                                                                                       \
    {                                                                                                        \
        asm volatile("s_waitcnt lgkmcnt(0)" ::: "memory");                                                   \
        glds4(ws + OFF_ST2 + (2 * grp + (hf)) * 4096, lane * 16, (u32)(size_t)smem + L_RED + w * 6144);      \
        glds8(ws + OFF_X2 + grp * 65536 + (hf) * 32768 + w * 8192, lane * 16, ldsb + L_X + (hf) * 32768);    \
    }
#pragma unroll
        for (int hf = 0; hf < 2; ++hf) {
            STAMP(16 + layer * 24 + hf * 5);
            if (layer == 1) {
                v4u ys[8], xs[8];
                LAS char* xp = smem + L_X + hf * 32768 + w * 8192 + lane * 16;
#pragma unroll
                for (int i = 0; i < 8; ++i) ys[i] = *(LAS v4u*)(xp + L_Y + i * 1024);
                if (hf == 0) VMW(3); else VMW(6);
                STAMP(17 + layer * 24 + hf * 5);
                f32x2 pst[8];
#pragma unroll
                for (int i = 0; i < 8; ++i) pst[i] = *(LAS f32x2*)(smem + L_RED + w * 6144 + lane * 8 + i * 512);
#pragma unroll
                for (int i = 0; i < 8; ++i) xs[i] = *(LAS v4u*)(xp + i * 1024);
                SB();
                f32x2 s2 = ((pst[0] + pst[1]) + (pst[2] + pst[3])) + ((pst[4] + pst[5]) + (pst[6] + pst[7]));
                const float mean = xadd32(s2.x) * (1.f / 512.f);
                const float rs = __builtin_amdgcn_rsqf(fmaxf(xadd32(s2.y) * (1.f / 512.f) - mean * mean, 0.f) + 1e-5f);
                LAS float* st = (LAS float*)(smem + L_MS + w * 256);
                st[lane] = lane < 32 ? rs : -mean * rs;
                const int tok0 = 16 * (((lane >> 1) ^ g) & 1) + 8 * (lane & 1);
                const f32x4 a0 = *(LAS f32x4*)(st + tok0), a1 = *(LAS f32x4*)(st + tok0 + 4);
                const f32x4 b0 = *(LAS f32x4*)(st + 32 + tok0), b1 = *(LAS f32x4*)(st + 36 + tok0);
                SB();
#pragma unroll
                for (int i = 0; i < 8; ++i) {
                    v4u o;
                    f32x2 x;
                    x = up2(xs[i].x); o.x = pk2(fma2(fma2(x, a0.xy, b0.xy), up2(ys[i].x), x));
                    x = up2(xs[i].y); o.y = pk2(fma2(fma2(x, a0.zw, b0.zw), up2(ys[i].y), x));
                    x = up2(xs[i].z); o.z = pk2(fma2(fma2(x, a1.xy, b1.xy), up2(ys[i].z), x));
                    x = up2(xs[i].w); o.w = pk2(fma2(fma2(x, a1.zw, b1.zw), up2(ys[i].w), x));
                    *(LAS v4u*)(xp + i * 1024) = o;
                    SB();
                }
            }
            if (layer == 1 || hf == 1) VMW(0);
            BAR();
            if (tid == 0) {
                if (hf == 1) __hip_atomic_store((GAS u64*)(ws + fgo + layer * 131072 + cg * 128), tag, RLX);
                else if (layer == 1) __hip_atomic_store((GAS u64*)(ws + fgo + 65536 + 32768 + cg * 128), tag, RLX);
            }
            if (layer == 0 && hf == 1) {
                glds8(ws + OFF_Y + grp * 65536 + w * 8192, lane * 16, ldsb + L_Y);
                glds8(ws + OFF_Y + grp * 65536 + 32768 + w * 8192, lane * 16, ldsb + L_Y + 32768);
            }
            STAMP(18 + layer * 24 + hf * 5);
#pragma unroll
            for (int kk = 0; kk < 16; ++kk)
#pragma unroll
                for (int j = 0; j < 3; ++j) { if (kk * 3 + j < NA1) PIN_A(W1[kk][j]); else PIN_V(W1[kk][j]); }
            const LAS char* xb[2][2];
#pragma unroll
            for (int i = 0; i < 2; ++i) {
                xb[0][i] = smem + L_X + hf * 32768 + (4 * g + (c >> 2)) * 64 + (((i ^ w ^ g) & 1) << 5) + 8 * (c & 3) + xrot;
                xb[1][i] = xb[0][i] - 2 * xrot;
            }
            f32x4 acc[3][2], accS = {0.f, 0.f, 0.f, 0.f}, accG = {0.f, 0.f, 0.f, 0.f};
#pragma unroll
            for (int j = 0; j < 3; ++j)
#pragma unroll
                for (int i = 0; i < 2; ++i) acc[j][i] = f32x4{0.f, 0.f, 0.f, 0.f};
            v4u X[3][2];
#define G1_LOAD(kk, i)                                                                   \
    {                                                                                    \
        const v2u lo = tr_read(xb[(kk) >> 3][i] + (kk) * 2048), hi = tr_read(xb[(kk) >> 3][i] + (kk) * 2048 + 1024); \
        X[(kk) % 3][i] = v4u{lo.x, lo.y, hi.x, hi.y};                                    \
    }
            G1_LOAD(0, 0) G1_LOAD(0, 1) G1_LOAD(1, 0) G1_LOAD(1, 1)
            SB();
            const v4u ones = {0x3f803f80u, 0x3f803f80u, 0x3f803f80u, 0x3f803f80u};
            LAS f32x2* ls = (LAS f32x2*)(smem + L_ST + hf * 512);
            f32x2 s2[2][2];
            f32x4 ct[3];
            float rs[2], nb[2];
#pragma unroll
            for (int kk = 0; kk < 16; ++kk) {
                acc[0][0] = mfma(W1[kk][0], X[kk % 3][0], acc[0][0]);
                if (kk < 14) G1_LOAD(kk + 2, 0)
                SB();
                acc[1][0] = mfma(W1[kk][1], X[kk % 3][0], acc[1][0]);
                if (kk < 14) G1_LOAD(kk + 2, 1)
                SB();
                acc[2][0] = mfma(W1[kk][2], X[kk % 3][0], acc[2][0]);
                if (kk < 8) {
                    accS = mfma(ones, X[kk % 3][0], accS);
                    accG = mfma(X[kk % 3][0], X[kk % 3][0], accG);
                }
                if (kk == 9) {
                    const float gd = (c & 2) ? ((c & 1) ? accG[3] : accG[2]) : ((c & 1) ? accG[1] : accG[0]);
                    if (g == (c >> 2)) ls[w * 16 + c] = f32x2{accS[0], gd};
                }
                if (kk == 11) {
#pragma unroll
                    for (int i = 0; i < 2; ++i) {
                        s2[i][0] = ls[((i ^ w) & 1) * 16 + c];
                        s2[i][1] = ls[(((i ^ w) & 1) + 2) * 16 + c];
                    }
#pragma unroll
                    for (int j = 0; j < 3; ++j) ct[j] = *(const LAS f32x4*)(smem + L_CS + (w * 3 + j) * 64 + g * 16);
                }
                if (kk == 13) {
#pragma unroll
                    for (int i = 1; i >= 0; --i) {
                        const f32x2 s = s2[i][0] + s2[i][1];
                        const float mean = s.x * (1.f / 512.f);
                        rs[i] = __builtin_amdgcn_rsqf(fmaxf(s.y * (1.f / 512.f) - mean * mean, 0.f) + 1e-5f);
                        nb[i] = -mean * rs[i];
                    }
                }
                if (kk == 12 && (hf == 1 || layer == 1)) fl = LD8S(rws, fvo, hf == 1 ? fgo + layer * 131072 : fgo + 65536 + 32768);
#pragma unroll
                for (int j = 0; j < 3; ++j) acc[j][1] = mfma(W1[kk][j], X[kk % 3][1], acc[j][1]);
                if (kk == 10) BAR();
                SB();
            }
            STAMP(19 + layer * 24 + hf * 5);
            if (hf == 0 && layer == 1) {
                POLL_FINISH(fl, fvo, fgo + 65536 + 32768);
                X2_REQUEST(1)
            }
#pragma unroll
            for (int i = 0; i < 2; ++i)
#pragma unroll
                for (int j = 0; j < 3; ++j) {
                    const int T = T0 + j;
                    const f32x4 u = acc[j][i] * rs[i] + ct[j] * nb[i];
                    const s16x2 z = {0, 0};
                    const v2u o = {__builtin_bit_cast(u32, __builtin_elementwise_max(__builtin_bit_cast(s16x2, pk2(u.xy)), z)),
                                   __builtin_bit_cast(u32, __builtin_elementwise_max(__builtin_bit_cast(s16x2, pk2(u.zw)), z))};
                    ST8S(o, rws, T < Tend ? (((i ^ w) & 1) * 1024 + lane * 16) : OOB,
                         hoff + hf * NP * 2048 + (T >> 1) * 2048 + 8 * (T & 1));
                }
            SB();
            STAMP(20 + layer * 24 + hf * 5);
        }
        POLL_FINISH(fl, fvo, fgo + layer * 131072);
#pragma unroll
        for (int s = 0; s < 8; ++s) H_LOAD(0, s)
        SB();
        STAMP(26 + layer * 24);
#pragma unroll
        for (int hf = 0; hf < 2; ++hf) {
#pragma unroll
            for (int s = 0; s < 11; ++s)
#pragma unroll
                for (int j = 0; j < 4; ++j) PIN_A(W2[s][j]);
            f32x4 acc[2][4];
#pragma unroll
            for (int i = 0; i < 2; ++i)
#pragma unroll
                for (int j = 0; j < 4; ++j) acc[i][j] = f32x4{0.f, 0.f, 0.f, 0.f};
            const int tt = w & 1, chl = 32 * (w >> 1) + 2 * c;
            const int xo = (64 * cg + chl) * 64 + ((tt ^ ((c >> 1) & 1)) << 5) + 8 * g;
            v2u res[2];
#pragma unroll
            for (int s = 0; s < 11; ++s) {
#pragma unroll
                for (int i = 0; i < 2; ++i)
#pragma unroll
                    for (int j = 0; j < 4; ++j) acc[i][j] = mfma(H[s & 7][i], W2[s][j], acc[i][j]);
                SB();
                if (s == (hf == 0 ? 0 : 4)) {
                    BAR();
                    if (tid == 0) {
                        if (hf == 0) __hip_atomic_store((GAS u64*)(ws + fgo + layer * 131072 + 32768 + cg * 128), tag, RLX);
                        else if (layer == 0) __hip_atomic_store((GAS u64*)(ws + fgo + 65536 + cg * 128), tag, RLX);
                    }
                }
                if (s < 3) H_LOAD(hf, s + 8)
                if (s == 2 && hf == 0) fl = LD8S(rws, fvo, fgo + layer * 131072 + 32768);
                if (s == 8) {
                    res[0] = *(const LAS v2u*)(smem + L_X + hf * 32768 + xo);
                    res[1] = *(const LAS v2u*)(smem + L_X + hf * 32768 + xo + 64);
                }
                if (s == 9 && hf == 1 && layer == 0) fl = LD8S(rws, fvo, fgo + 65536);
                SB();
            }
            STAMP(27 + layer * 24 + hf * 4);
            if (hf == 0) {
                POLL_FINISH(fl, fvo, fgo + layer * 131072 + 32768);
#pragma unroll
                for (int s = 0; s < 4; ++s) H_LOAD(1, s)
                SB();
            }
            STAMP(28 + layer * 24 + hf * 4);
            LAS f32x4* red = (LAS f32x4*)(smem + L_RED);
#pragma unroll
            for (int i = 0; i < 2; ++i)
#pragma unroll
                for (int j = 0; j < 4; ++j) {
                    const int src = i + 2 * (j >> 1);
                    if (src) red[(((w ^ src) * 3 + src - 1) * 2 + (j & 1)) * 64 + lane] = acc[i][j];
                }
            BAR();
            f32x4 rd[6];
#pragma unroll
            for (int i = 0; i < 6; ++i) rd[i] = red[(w * 6 + i) * 64 + lane];
            SB();
            if (hf == 1 && layer == 0) {
                POLL_FINISH(fl, fvo, fgo + 65536);
                X2_REQUEST(0)
            }
            STAMP(29 + layer * 24 + hf * 4);
            f32x2 xf[2][2];
#pragma unroll
            for (int j = 0; j < 2; ++j) {
                const f32x4 t = (acc[0][j] + rd[j]) + (rd[2 + j] + rd[4 + j]);
                xf[j][0] = up2(res[j].x) + t.xy;
                xf[j][1] = up2(res[j].y) + t.zw;
            }
            if (layer == 1) {
#pragma unroll
                for (int r = 0; r < 4; ++r)
                    __builtin_amdgcn_raw_buffer_store_b32(pk2bf(xf[0][r >> 1][r & 1], xf[1][r >> 1][r & 1]), mkrs(out, 2 * MB),
                                                          (16 * tt + 4 * g + r) * 1024 + chl * 2, (64 * grp + 32 * hf) * 1024 + cg * 128, 0);
            } else {
                u32 xq[2][2];
#pragma unroll
                for (int j = 0; j < 2; ++j) {
                    xq[j][0] = pk2(xf[j][0]);
                    xq[j][1] = pk2(xf[j][1]);
                    ST8S((v2u{xq[j][0], xq[j][1]}), rws, xo + 64 * j, OFF_X2 + (2 * grp + hf) * 32768);
                }
                f32x2 sm[2], sq[2];
#pragma unroll
                for (int r = 0; r < 2; ++r) {
                    const f32x2 x0 = up2(xq[0][r]), x1 = up2(xq[1][r]);
                    sm[r] = x0 + x1;
                    sq[r] = fma2(x0, x0, x1 * x1);
                }
                f32x4 s4;
                {
                    const bool odd = c & 1;
                    const f32x4 keep = odd ? f32x4{sm[1].x, sq[1].x, sm[1].y, sq[1].y} : f32x4{sm[0].x, sq[0].x, sm[0].y, sq[0].y};
                    const f32x4 give = odd ? f32x4{sm[0].x, sq[0].x, sm[0].y, sq[0].y} : f32x4{sm[1].x, sq[1].x, sm[1].y, sq[1].y};
#pragma unroll
                    for (int e = 0; e < 4; ++e) {
                        const float gv = give[e];
                        float t = keep[e] + __builtin_bit_cast(float, __builtin_amdgcn_update_dpp(0, __builtin_bit_cast(int, gv), 0xb1, 0xf, 0xf, true));
                        t = dppadd<0x122>(t);
                        t = dppadd<0x124>(t);
                        t = dppadd<0x128>(t);
                        s4[e] = t;
                    }
                }
                ST16S(__builtin_bit_cast(v4u, s4), rws, c < 2 ? (cg * 2 + (w >> 1)) * 256 + (16 * tt + 4 * g + 2 * c) * 8 : OOB,
                      OFF_ST2 + (2 * grp + hf) * 4096);
            }
            SB();
            if (hf == 0) {
#pragma unroll
                for (int s = 4; s < 8; ++s) H_LOAD(1, s)
                SB();
            }
            STAMP(30 + layer * 24 + hf * 4);
        }
    }
}

extern "C" void kernel_launch(void* const* d_in, const int* in_sizes, int n_in, void* d_out, int out_size, void* d_ws,
                              size_t ws_size, hipStream_t stream) {
    constexpr int lds = L_BYTES;
    (void)hipFuncSetAttribute((const void*)k_all, hipFuncAttributeMaxDynamicSharedMemorySize, lds);
    k_all<<<256, 256, lds, stream>>>((const u16*)d_in[0], (const u16*)d_in[1], (const u16*)d_in[2], (const u16*)d_in[3],
                                     (const u16*)d_in[4], (const u16*)d_in[5], (const u16*)d_in[7], (const u16*)d_in[8],
                                     (u16*)d_out, (char*)d_ws);
}
```

```cpp
#include <hip/hip_runtime.h>
#include <hip/hip_bf16.h>
#include <stdint.h>

typedef unsigned short u16;
typedef unsigned int u32;
typedef unsigned long long u64;
typedef u32 v4u __attribute__((ext_vector_type(4)));
typedef u32 v2u __attribute__((ext_vector_type(2)));
typedef float f32x4 __attribute__((ext_vector_type(4)));
typedef float f32x2 __attribute__((ext_vector_type(2)));
typedef short bf16x8 __attribute__((ext_vector_type(8)));
typedef short s16x4 __attribute__((ext_vector_type(4)));
typedef short s16x2 __attribute__((ext_vector_type(2)));
typedef __bf16 bf16x2 __attribute__((ext_vector_type(2)));
#define LAS __attribute__((address_space(3)))
#define GAS __attribute__((address_space(1)))
#define DEV __device__ __forceinline__
#define RLX __ATOMIC_RELAXED, __HIP_MEMORY_SCOPE_AGENT
typedef __amdgpu_buffer_rsrc_t rsrc_t;

constexpr int S_ = 2048, D_ = 512, F_ = 1376;
constexpr int NP = 43;
constexpr int NT = 86;
constexpr int MB = 1 << 20;
constexpr int OFF_FL = 0, OFF_ST0 = 512 * 1024, OFF_ST2 = 640 * 1024;
constexpr int OFF_UR = 1 * MB, OFF_X0T = 3 * MB, OFF_Y = 5 * MB, OFF_X1 = 7 * MB, OFF_X2 = 9 * MB, OFF_W1T = 11 * MB,
              OFF_W2T = 13 * MB, OFF_H = 15 * MB, H_LAYER = 6 * MB, WS_USED = 27 * MB;
constexpr int FL_EPOCH = 0, FL_A = 4096, FL_A2 = 40960, FL_B = 49152, FL_W = 57344, FL_G = 81920;
constexpr int SC1 = 16, OOB = 0x40000000;
constexpr int L_X = 0, L_Y = 65536, L_W = 98304, L_RED = 131072, L_ST = 155648, L_MS = L_ST + 1024, L_CS = L_MS + 1024, L_BYTES = L_CS + 1024;
constexpr int CV_E = 0, CV_O = 4160, CV_U = 8320, CV_CH = 12928, CV_X = 2 * CV_CH, L_P0 = 49152;
constexpr u32 SPIN_CAP = 1u << 18;
#define NA1 8
#define HR 8
#define PSLEEP 2
#define SECMODE 0
#define QAUX 2
#define NTAUX 2
#define WAUX 2
#define A2R 1
#define A1DELAY 12
#define WPOS 1
#define NW_B 34
#define NW2_A 0
#define NW2_B 20
#define NPRE 2
#define CPOLL 9
#define CPOLL2 8
#define DPOLL 3
#define DCHK 9
#define MIDMIX 3
#define CODEPF 1
#define NW_CONV 10
#define NW2_PRE 0

static_assert(NW_CONV <= 12 && NW_CONV <= NW_B && NW_B <= 40, "slots");
DEV float lo2f(u32 d) { return __builtin_bit_cast(float, d << 16); }
DEV float hi2f(u32 d) { return __builtin_bit_cast(float, d & 0xffff0000u); }
DEV f32x2 up2(u32 d) { return f32x2{lo2f(d), hi2f(d)}; }
DEV u32 pk2(f32x2 x) { return __builtin_bit_cast(u32, __builtin_convertvector(x, bf16x2)); }
DEV u32 pk2bf(float a, float b) { return pk2(f32x2{a, b}); }
DEV f32x2 fma2(f32x2 a, f32x2 b, f32x2 c) { return __builtin_elementwise_fma(a, b, c); }
DEV f32x4 mfma(v4u a, v4u b, f32x4 c) {
    return __builtin_amdgcn_mfma_f32_16x16x32_bf16(__builtin_bit_cast(bf16x8, a), __builtin_bit_cast(bf16x8, b), c, 0, 0, 0);
}
DEV v2u tr_read(const LAS char* p) {
    return __builtin_bit_cast(v2u, __builtin_amdgcn_ds_read_tr16_b64_v4i16((LAS s16x4*)p));
}
DEV float xadd16(float v) {
    auto r = __builtin_amdgcn_permlane16_swap(__builtin_bit_cast(u32, v), __builtin_bit_cast(u32, v), false, false);
    const u32 r0 = r[0], r1 = r[1];
    return __builtin_bit_cast(float, r0) + __builtin_bit_cast(float, r1);
}
DEV float xadd32(float v) {
    auto r = __builtin_amdgcn_permlane32_swap(__builtin_bit_cast(u32, v), __builtin_bit_cast(u32, v), false, false);
    const u32 r0 = r[0], r1 = r[1];
    return __builtin_bit_cast(float, r0) + __builtin_bit_cast(float, r1);
}
template <int CTRL> DEV float dppadd(float v) {
    return v + __builtin_bit_cast(float, __builtin_amdgcn_update_dpp(0, __builtin_bit_cast(int, v), CTRL, 0xf, 0xf, true));
}
DEV float row16sum(float v) {
    v = dppadd<0x121>(v);
    v = dppadd<0x122>(v);
    v = dppadd<0x124>(v);
    v = dppadd<0x128>(v);
    return v;
}
DEV rsrc_t mkrs(const void* p, int bytes) { return __builtin_amdgcn_make_buffer_rsrc((void*)p, 0, bytes, 0x00020000); }
#define LD16(r, vo, so) __builtin_amdgcn_raw_buffer_load_b128(r, vo, so, QAUX)
#define LD16N(r, vo, so) __builtin_amdgcn_raw_buffer_load_b128(r, vo, so, NTAUX)
#define LD4(r, vo, so) __builtin_amdgcn_raw_buffer_load_b32(r, vo, so, WAUX)
#define LD16S(r, vo, so) __builtin_amdgcn_raw_buffer_load_b128(r, vo, so, SC1)
#define LD8S(r, vo, so) __builtin_amdgcn_raw_buffer_load_b64(r, vo, so, SC1)
#define ST16S(v, r, vo, so) __builtin_amdgcn_raw_buffer_store_b128(v, r, (vo) + (so), 0, SC1)
#define ST8S(v, r, vo, so) __builtin_amdgcn_raw_buffer_store_b64(v, r, vo, so, SC1)
#define SB() __builtin_amdgcn_sched_barrier(0)
#define PIN_A(x) asm volatile("" : "+a"(x))
#define PIN_V(x) asm volatile("" : "+v"(x))
#define BAR() do { SB(); asm volatile("s_waitcnt lgkmcnt(0)\n\ts_barrier" ::: "memory"); SB(); } while (0)
#define VMW(n) do { SB(); asm volatile("s_waitcnt vmcnt(%0)" ::"n"(n) : "memory"); SB(); } while (0)
DEV void glds8(const char* sbase, u32 voff, u32 lds) {
    u32 keep;
    asm volatile(
        "s_mov_b32 %0, m0\n\ts_mov_b32 m0, %3\n\ts_nop 0\n\t"
        "global_load_lds_dwordx4 %1, %2 offset:-4096 sc1\n\t"
        "global_load_lds_dwordx4 %1, %2 offset:-3072 sc1\n\t"
        "global_load_lds_dwordx4 %1, %2 offset:-2048 sc1\n\t"
        "global_load_lds_dwordx4 %1, %2 offset:-1024 sc1\n\t"
        "global_load_lds_dwordx4 %1, %2 sc1\n\t"
        "global_load_lds_dwordx4 %1, %2 offset:1024 sc1\n\t"
        "global_load_lds_dwordx4 %1, %2 offset:2048 sc1\n\t"
        "global_load_lds_dwordx4 %1, %2 offset:3072 sc1\n\t"
        "s_mov_b32 m0, %0"
        : "=&s"(keep) : "v"(voff), "s"(sbase + 4096), "s"(lds + 4096) : "memory");
}
DEV void glds4(const char* sbase, u32 voff, u32 lds) {
    u32 keep;
    asm volatile(
        "s_mov_b32 %0, m0\n\ts_mov_b32 m0, %3\n\ts_nop 0\n\t"
        "global_load_lds_dwordx4 %1, %2 sc1\n\t"
        "global_load_lds_dwordx4 %1, %2 offset:1024 sc1\n\t"
        "global_load_lds_dwordx4 %1, %2 offset:2048 sc1\n\t"
        "global_load_lds_dwordx4 %1, %2 offset:3072 sc1\n\t"
        "s_mov_b32 m0, %0"
        : "=&s"(keep) : "v"(voff), "s"(sbase), "s"(lds) : "memory");
}
DEV u32 mktag(u32 e) { return (u32)__builtin_amdgcn_readfirstlane(e) * 1664525u + 1013904223u; }
#define LD4S(vo, so) __builtin_amdgcn_raw_buffer_load_b32(rws, vo, so, SC1)
#define FLAG(vo, so) __builtin_amdgcn_raw_buffer_store_b32(tag, rws, vo, so, SC1)
#define POLL_FINISH(f, vo, so)                                            \
    do {                                                                  \
        if (!__all(f == tag)) {                                           \
            u32 spins_ = 0;                                               \
            bool ok_ = f == tag;                                          \
            while (!dead) {                                               \
                __builtin_amdgcn_s_sleep(PSLEEP);                         \
                asm volatile("" ::: "memory");                            \
                const u32 g_ = LD4S(ok_ ? OOB : (vo), so);                \
                ok_ = ok_ || g_ == tag;                                   \
                if (__all(ok_)) break;                                    \
                if (++spins_ > SPIN_CAP) dead = true;                     \
            }                                                             \
        }                                                                 \
        __builtin_amdgcn_fence(__ATOMIC_ACQUIRE, "wavefront");            \
        SB();                                                             \
    } while (0)
#define ALLOK(P) __all(P.x == tag && P.y == tag && P.z == tag && P.w == tag)
#define POLL_ALL(f, so)                                                   \
    do {                                                                  \
        if (!ALLOK(f)) {                                                  \
            u32 spins_ = 0;                                               \
            while (!dead) {                                               \
                __builtin_amdgcn_s_sleep(8);                              \
                asm volatile("" ::: "memory");                            \
                const v4u g_ = LD16S(rws, lane * 16, so);                 \
                if (ALLOK(g_)) break;                                     \
                if (++spins_ > SPIN_CAP) dead = true;                     \
            }                                                             \
        }                                                                 \
        __builtin_amdgcn_fence(__ATOMIC_ACQUIRE, "wavefront");            \
        SB();                                                             \
    } while (0)
#define STAMP(i)

__global__ __launch_bounds__(256, 1) void k_all(const u16* __restrict__ q, const u16* __restrict__ k, const u16* __restrict__ v,
                                                const u16* __restrict__ sk, const u16* __restrict__ ssmk,
                                                const u16* __restrict__ Dv, const u16* __restrict__ w1,
                                                const u16* __restrict__ w2, u16* __restrict__ out, char* __restrict__ ws) {
    extern __shared__ __attribute__((aligned(16))) char smem_[];
    LAS char* smem = (LAS char*)smem_;
    const int b = blockIdx.x, tid = threadIdx.x, w = __builtin_amdgcn_readfirstlane(tid >> 6);
    const int xc = b & 7, jb = b >> 3;
    const rsrc_t rws = mkrs(ws, WS_USED);
    bool dead = false;
    STAMP(0);
    const int ch0 = 64 * xc + 2 * jb;
    const int grp = 4 * xc + (b >> 6), cg = jb & 7;
    const int T0 = 11 * cg + 3 * w, Tend = cg == 7 ? NT : 11 * cg + 11;
    u32 tag;
    v4u W1[40], W2[11][4];
    {
        const int lane = tid & 63, c = lane & 15, g = lane >> 4;
#define Q_LOADS()                                                                                              \
    _Pragma("unroll") for (int i = 0; i < 4; ++i) hq[i] = LD16(r2, (w < 2 ? lane * 16 : qvo) + i * 1024, 0);   \
    _Pragma("unroll") for (int i = 0; i < 4; ++i) xr[i] = LD16(r2, (8 * b + 4 * half + 4 + i) * 1024 + lane * 16 - (w < 2 ? 0 : 4096), 0);
#define W_LOADS()                                                                                              \
    _Pragma("unroll") for (int i = 0; i < 3; ++i) {                                                            \
        const int hu = i * 512 + 2 * b + half;                                                                 \
        const bool is1 = hu < 688;                                                                             \
        const int p = (is1 ? hu : hu - 688) >> 4, r = hu & 15;                                                 \
        const rsrc_t rw = mkrs(is1 ? w1 : w2, hu < 1376 && w >= 2 ? F_ * D_ * 2 : 0);                          \
        const int wvo = is1 ? ((32 * r + 4 * g) * F_ + 32 * p + 2 * c) * 2 : ((32 * p + 16 * (g & 1) + (g >> 1)) * D_ + 32 * r + 2 * c) * 2; \
        const int s2 = is1 ? 16 * F_ * 2 : 8 * D_ * 2, s1 = is1 ? F_ * 2 : 2 * D_ * 2;                             \
        _Pragma("unroll") for (int e = 0; e < 8; ++e) wd[i][e] = LD4(rw, wvo, (e >> 2) * s2 + (e & 3) * s1);   \
        _Pragma("unroll") for (int u = 0; u < 2; ++u) {                                                        \
            const int T = 2 * p + u, tc = T / 11, tr = T - 11 * tc, tw = tr / 3, tj = tr - 3 * tw;             \
            const int o1 = OFF_W1T + (tc * 4 + tw) * 49152 + (((r - 8 * (tw >> 1)) & 15) * 3 + tj) * 1024;     \
            const int o2 = OFF_W2T + ((r >> 1) * NP + p) * 4096 + ((r & 1) * 2 + u) * 1024;                    \
            wdst[i][u] = hu < 1376 ? (is1 ? o1 : o2) : OOB;                                                    \
        }                                                                                                      \
    }
        u32 pf;
        {
            u64 pc;
            u32 len;
            asm volatile("s_getpc_b64 %0\n.Lpf_base:\n\ts_mov_b32 %1, .Lpf_end-.Lpf_base" : "=s"(pc), "=s"(len));
            pf = __builtin_amdgcn_raw_buffer_load_b32(mkrs((const void*)pc, len), w == 3 && lane < 8 ? jb * 1024 + lane * 128 : OOB, 0, 0);
        }
        const int cw = w >> 1, half = w & 1, ch = ch0 + cw;
        u32 draw;
        {
            const int row = 2 * (tid >> 3), s0 = 64 * jb + row;
            const int vo = s0 * 1024 + xc * 128 + (lane & 7) * 16;
            const rsrc_t rk = mkrs(k, 2 * MB), rv = mkrs(v, 2 * MB);
            v4u kk[5], vv[2], sw[4];
            u32 wd[3][8];
            int wdst[3][2];
#pragma unroll
            for (int t = 0; t < 5; ++t) kk[t] = LD16N(rk, s0 + t - 3 >= 0 ? vo + (t - 3) * 1024 : OOB, 0);
#pragma unroll
            for (int t = 0; t < 2; ++t) vv[t] = LD16N(rv, vo + t * 1024, 0);
#pragma unroll
            for (int t = 0; t < 4; ++t) sw[t] = *(const v4u*)(sk + (64 * xc + 8 * (lane & 7)) * 4 + 8 * t);
            const u32 epoch = LD4S(0, OFF_FL + FL_EPOCH);
            draw = __builtin_amdgcn_raw_buffer_load_b16(mkrs(Dv, 1024), 0, ch * 2, 0);
            const rsrc_t r2 = mkrs(w < 2 ? ssmk + (ch0 + half) * S_ : q, w < 2 ? 4096 : 2 * MB);
            const int qvo = (64 * jb + 32 * half + 4 * (lane >> 3)) * 1024 + xc * 128 + (lane & 7) * 16;
            v4u hq[4], xr[4];
            W_LOADS()
            Q_LOADS()
            SB();
#define W_STORES()                                                                                          \
    _Pragma("unroll") for (int i = 0; i < 6; ++i) {                                                         \
        v4u fr;                                                                                             \
        _Pragma("unroll") for (int e = 0; e < 4; ++e)                                                       \
            fr[e] = __builtin_amdgcn_perm(wd[i >> 1][2 * e + 1], wd[i >> 1][2 * e], (i & 1) ? 0x07060302u : 0x05040100u); \
        ST16S(fr, rws, lane * 16, wdst[i >> 1][i & 1]);                                                     \
    }
#pragma unroll
            for (int t = 0; t < 2; ++t) {
                v4u o;
#pragma unroll
                for (int j = 0; j < 4; ++j) {
                    f32x2 a = up2(kk[t][j]) * f32x2{hi2f(sw[j].y), hi2f(sw[j].w)};
                    a = fma2(up2(kk[t + 1][j]), f32x2{lo2f(sw[j].y), lo2f(sw[j].w)}, a);
                    a = fma2(up2(kk[t + 2][j]), f32x2{hi2f(sw[j].x), hi2f(sw[j].z)}, a);
                    a = fma2(up2(kk[t + 3][j]), f32x2{lo2f(sw[j].x), lo2f(sw[j].z)}, a);
                    o[j] = pk2(a * up2(vv[t][j]));
                }
                *(LAS v4u*)(smem + L_P0 + (row + t) * 128 + (lane & 7) * 16) = o;
            }
            tag = mktag(epoch);
            PIN_V(draw);
            PIN_V(pf);
            BAR();
            STAMP(1);
            const int qd = c >> 2;
            if (__builtin_expect(w < 2, 1)) {
                const LAS char* base = smem + L_P0 + 64 * half + 8 * (c & 3) + (63 - 8 * g - qd) * 128;
                const int dst = (64 * xc + 32 * half + c) * 4096 + 16 * g + OFF_UR + (1984 - 64 * jb) * 2;
                v2u a[8];
#pragma unroll
                for (int i = 0; i < 8; ++i) a[i] = tr_read(base + (i >> 2) * 32 - (32 * ((i >> 1) & 1) + 4 * (i & 1)) * 128);
#pragma unroll
                for (int i = 0; i < 4; ++i)
                    ST16S((v4u{a[2 * i].x, a[2 * i].y, a[2 * i + 1].x, a[2 * i + 1].y}), rws, dst, (i & 1) * 64 + (i >> 1) * 65536);
                SB();
                LAS char* cv = smem + half * CV_CH;
                const v4u zero = {0, 0, 0, 0};
                if (lane < 33) *(LAS v4u*)(cv + (lane < 30 ? CV_U + 4096 + lane * 16 : lane < 32 ? CV_E + 16 * (lane - 30) : CV_O)) = zero;
                if (lane == 0) *(LAS v4u*)(cv + CV_O + 16) = v4u{0, 0, 0, hq[0].x << 16};
#pragma unroll
                for (int i = 0; i < 4; ++i) {
                    const u32 nx = i < 3 ? __builtin_amdgcn_readlane(hq[i < 3 ? i + 1 : i].x, 0) : 0u;
                    v4u o;
                    o.x = __builtin_amdgcn_alignbyte(hq[i].y, hq[i].x, 2);
                    o.y = __builtin_amdgcn_alignbyte(hq[i].z, hq[i].y, 2);
                    o.z = __builtin_amdgcn_alignbyte(hq[i].w, hq[i].z, 2);
                    o.w = __builtin_amdgcn_alignbyte((u32)__builtin_amdgcn_update_dpp((int)nx, (int)hq[i].x, 0x130, 0xf, 0xf, false), hq[i].w, 2);
                    *(LAS v4u*)(cv + CV_E + 32 + lane * 16 + i * 1024) = hq[i];
                    *(LAS v4u*)(cv + CV_O + 32 + lane * 16 + i * 1024) = o;
                }
                VMW(0);
#pragma unroll
                for (int i = 0; i < 4; ++i) PIN_V(xr[i]);
                STAMP(2);
                const int fa1 = tid < 8 ? FL_W + tid * 1024 + b * 4 : tid == 8 ? FL_A + (xc * 32 + jb) * 128 : OOB;
                BAR();
                FLAG(fa1, OFF_FL);
                if (w == 0) {
                    const int fo = OFF_FL + FL_A + xc * 4096, fv = lane < 32 ? lane * 128 : OOB;
                    __builtin_amdgcn_s_sleep(A1DELAY);
                    u32 f = LD4S(fv, fo);
                    if (lane >= 32) f = tag;
                    POLL_FINISH(f, fv, fo);
                    STAMP(5);
                    glds4(ws + OFF_UR + ch0 * 4096, lane * 16, (u32)(size_t)(smem + CV_U));
                    glds4(ws + OFF_UR + ch0 * 4096 + 4096, lane * 16, (u32)(size_t)(smem + CV_CH + CV_U));
                }
            } else {
                W_STORES()
                SB();
                LAS char* img = smem + L_P0 + 8192 + half * 4096;
#pragma unroll
                for (int t = 0; t < 4; ++t) *(LAS v4u*)(img + (4 * (lane >> 3) + t) * 128 + (lane & 7) * 16) = hq[t];
                v2u a[8];
#pragma unroll
                for (int i = 0; i < 8; ++i) a[i] = tr_read(img + 8 * (c & 3) + (i >> 1) * 32 + (4 * (i & 1) + 8 * g + qd) * 128);
                f32x4 sta, stb;
#pragma unroll
                for (int i = 0; i < 4; ++i) {
                    f32x2 x[4];
#pragma unroll
                    for (int j = 0; j < 4; ++j) x[j] = up2(xr[i][j]);
                    const f32x2 s2v = (x[0] + x[1]) + (x[2] + x[3]);
                    const float mean = xadd32(xadd16(row16sum(s2v.x + s2v.y))) * (1.f / 512.f);
                    f32x2 sq = {0.f, 0.f};
#pragma unroll
                    for (int j = 0; j < 4; ++j) sq = fma2(x[j] - mean, x[j] - mean, sq);
                    const float rs = __builtin_amdgcn_rsqf(xadd32(xadd16(row16sum(sq.x + sq.y))) * (1.f / 512.f) + 1e-5f);
                    sta[i] = rs;
                    stb[i] = -mean * rs;
                }
                v4u st4 = __builtin_bit_cast(v4u, lane ? stb : sta);
                PIN_V(st4);
                VMW(0);
                BAR();
                __builtin_amdgcn_s_sleep(1);
#pragma unroll
                for (int i = 0; i < 4; ++i)
                    ST16S((v4u{a[2 * i].x, a[2 * i].y, a[2 * i + 1].x, a[2 * i + 1].y}), rws, (64 * xc + c) * 4096 + 16 * g,
                          OFF_X0T + 128 * jb + 64 * half + i * 65536);
                ST16S(st4, rws, lane < 2 ? (8 * b + 4 * half) * 4 + lane * 8192 : OOB, OFF_ST0);
                STAMP(3);
                VMW(0);
                STAMP(4);
                volatile LAS u32* hs = (volatile LAS u32*)(smem + L_ST + 8);
                hs[half] = tag;
                FLAG(hs[half ^ 1] == tag && lane < 8 ? lane * 1024 + b * 4 : OOB, OFF_FL + FL_A2);
            }
        }
        const LAS char* ab = smem + cw * CV_CH + ((c & 1) ? CV_E + 2 * (1 + c) : CV_O + 2 * c) + 16 * g + 256 * half;
        const LAS char* bb = smem + cw * CV_CH + CV_U + 4064 - 3584 - 32 * c + 16 * g + 256 * half;
        v4u A[2][8], B[2][8];
#define CV_LA(buf, r, i) { const LAS u32* pa = (const LAS u32*)(ab + 512 * (i) + 64 * (r)); A[buf][i] = v4u{pa[0], pa[1], pa[2], pa[3]}; }
#define CV_LB(buf, r, i) B[buf][i] = *(const LAS v4u*)(bb + 512 * (7 - (i)) + 64 * (r));
        if (w == 0) VMW(0);
        BAR();
        STAMP(6);
        v4u fa, fw;
        CV_LA(0, 0, 0)
#pragma unroll
        for (int i = 0; i < 8; ++i) CV_LB(0, 0, i)
#pragma unroll
        for (int i = 1; i < 8; ++i) CV_LA(0, 0, i)
        f32x4 acc[8];
#pragma unroll
        for (int i = 0; i < 8; ++i) acc[i] = f32x4{0.f, 0.f, 0.f, 0.f};
        v2u x0[4], uu[4];
        f32x4 sa[4], sb[4];
        const int w1o = OFF_W1T + (cg * 4 + w) * 49152;
        int w1v[3];
#pragma unroll
        for (int j = 0; j < 3; ++j) w1v[j] = T0 + j < Tend ? j * 1024 + lane * 16 : OOB;
        const int tk = 1024 * half + 16 * c + 4 * g;
#define W1_LOAD(n) W1[n] = LD16S(rws, w1v[((n) + 8) % 3], w1o + (((n) + 8) / 3) * 3072)
#pragma unroll
        for (int r = 0; r < 4; ++r) {
            int idx = 0;
#pragma unroll
            for (int al = 0; al < 8; ++al)
#pragma unroll
                for (int be = 0; be + al < 8; ++be) {
                    acc[al + be] = mfma(A[r & 1][al], B[r & 1][be], acc[al + be]);
                    if (r < 3 && idx >= 4 && idx < 20) {
                        if (idx & 1) CV_LA((r + 1) & 1, r + 1, (idx - 4) >> 1) else CV_LB((r + 1) & 1, r + 1, (idx - 4) >> 1)
                    }
                    if (r == 3 && idx < 4) uu[idx] = *(const LAS v2u*)(smem + cw * CV_CH + CV_U + 2 * (2044 - tk) - 512 * idx);
                    if (r == 0 && idx == 4) fw = LD16S(rws, lane * 16, OFF_FL + FL_W + xc * 1024);
                    if (r == A2R && idx == 8) fa = LD16S(rws, lane * 16, OFF_FL + FL_A2 + xc * 1024);
                    if (r == A2R + 1 && idx % 3 == 2) {
                        const int n = idx / 3;
                        if (n < 4) x0[n] = LD8S(rws, tk * 2 + n * 512, OFF_X0T + ch * 4096);
                        else if (n < 8) sa[n - 4] = __builtin_bit_cast(f32x4, LD16S(rws, tk * 4 + (n - 4) * 1024, OFF_ST0));
                        else sb[n - 8] = __builtin_bit_cast(f32x4, LD16S(rws, tk * 4 + (n - 8) * 1024, OFF_ST0 + 8192));
                    }
                    if (r == (A2R == 0 ? 2 : 1) && idx % 3 == 1 && idx / 3 < NW_CONV) W1_LOAD(idx / 3);
                    SB();
                    ++idx;
                }
            if (r == 0) {
                POLL_ALL(fw, OFF_FL + FL_W + xc * 1024);
                glds8(ws + w1o, lane * 16, (u32)(size_t)smem + L_W + w * 8192);
            }
            if (r == A2R) {
                POLL_ALL(fa, OFF_FL + FL_A2 + xc * 1024);
                STAMP(7);
            }
        }
        STAMP(8);
        LAS f32x4* xch = (LAS f32x4*)(smem + CV_X);
        f32x4 fin[4];
        if (half) {
#pragma unroll
            for (int i = 0; i < 4; ++i) { xch[(w * 4 + i) * 64 + lane] = acc[i]; fin[i] = acc[4 + i]; }
        } else {
#pragma unroll
            for (int i = 0; i < 4; ++i) { xch[(w * 4 + i) * 64 + lane] = acc[4 + i]; fin[i] = acc[i]; }
        }
        BAR();
        {
            f32x4 oth[4];
#pragma unroll
            for (int i = 0; i < 4; ++i) oth[i] = xch[((w ^ 1) * 4 + i) * 64 + lane];
            SB();
            const float dval = lo2f(draw);
            const int off = (32 * half + (c >> 1)) * 32768 + ch * 64 + (((c & 1) ^ ((ch >> 2) & 1)) << 5) + 8 * g;
#pragma unroll
            for (int i = 0; i < 4; ++i) {
                const f32x4 y = fin[i] + oth[i];
                const f32x2 ya = fma2(f32x2{hi2f(uu[i].y), lo2f(uu[i].y)}, f32x2{dval, dval}, y.xy);
                const f32x2 yb = fma2(f32x2{hi2f(uu[i].x), lo2f(uu[i].x)}, f32x2{dval, dval}, y.zw);
                const f32x2 xa = up2(x0[i].x), xb2 = up2(x0[i].y);
                const f32x2 za = fma2(fma2(xa, sa[i].xy, sb[i].xy), ya, xa), zb = fma2(fma2(xb2, sa[i].zw, sb[i].zw), yb, xb2);
                ST8S((v2u{pk2(za), pk2(zb)}), rws, off, OFF_X1 + i * 8 * 32768);
                ST8S((v2u{pk2(ya), pk2(yb)}), rws, off, OFF_Y + i * 8 * 32768);
                SB();
            }
        }
        STAMP(9);
        const int fb1 = tid < 8 ? tid * 1024 + b * 4 : OOB;
        VMW(0);
        BAR();
        FLAG(fb1, OFF_FL + FL_B);
        STAMP(10);
        __builtin_amdgcn_s_sleep(1);
#pragma unroll
        for (int n = NW_CONV; n < NW_B; ++n) W1_LOAD(n);
        const int w2o = OFF_W2T + (cg * NP + w) * 4096, w2v = lane * 16 + 2048 * (w >> 1);
#define W2_LOAD(s, j) W2[s][j] = LD16S(rws, (s) == 10 && w == 3 ? OOB : (w2v ^ (((j) >> 1) * 2048)) + ((j) & 1) * 1024, w2o + (s) * 16384)
#pragma unroll
        for (int n = 0; n < NW2_PRE; ++n) W2_LOAD(n / 4, n % 4);
        const v4u fb = LD16S(rws, lane * 16, OFF_FL + FL_B + xc * 1024);
        SB();
        {
            const v4u ones = {0x3f803f80u, 0x3f803f80u, 0x3f803f80u, 0x3f803f80u};
            f32x4 cs[3];
#pragma unroll
            for (int j = 0; j < 3; ++j) cs[j] = f32x4{0.f, 0.f, 0.f, 0.f};
#pragma unroll
            for (int n = 0; n < 8; ++n) cs[n % 3] = mfma(*(const LAS v4u*)(smem + L_W + w * 8192 + n * 1024 + lane * 16), ones, cs[n % 3]);
#pragma unroll
            for (int n = 0; n < NW_B; ++n) {
                if (n < NA1) PIN_A(W1[n]); else PIN_V(W1[n]);
                cs[(n + 8) % 3] = mfma(W1[n], ones, cs[(n + 8) % 3]);
                SB();
            }
#pragma unroll
            for (int j = 0; j < 3; ++j) *(LAS f32x4*)(smem + L_CS + (w * 3 + j) * 64 + g * 16) = cs[j];
        }
        STAMP(11);
        POLL_ALL(fb, OFF_FL + FL_B + xc * 1024);
        STAMP(12);
        const u32 ldsb = (u32)(size_t)smem + w * 8192;
        glds8(ws + OFF_X1 + grp * 65536 + w * 8192, lane * 16, ldsb + L_X);
        glds8(ws + OFF_X1 + grp * 65536 + 32768 + w * 8192, lane * 16, ldsb + L_X + 32768);
#pragma unroll
        for (int n = NW_B; n < 40; ++n) W1_LOAD(n);
        FLAG(b == 0 && tid == 0 ? 0 : OOB, OFF_FL + FL_EPOCH);
        VMW(8 + 40 - NW_B + 1);
    }
#pragma unroll 1
    for (int layer = 0; layer < 2; ++layer) {
        int bl = blockIdx.x, w = __builtin_amdgcn_readfirstlane(tid >> 6);
        asm volatile("" : "+s"(bl), "+s"(w));
        const int grp = 4 * (bl & 7) + (bl >> 6), cg = (bl >> 3) & 7;
        const int T0 = 11 * cg + 3 * w, Tend = cg == 7 ? NT : 11 * cg + 11;
        const int fgo = OFF_FL + FL_G + grp * 1024;
        const u32 ldsb = (u32)(size_t)smem + w * 8192;
        const int xrot = 16384 * (w >> 1);
        const int hoff = OFF_H + layer * H_LAYER + 2 * grp * NP * 2048;
        int lane = tid & 63;
        PIN_V(lane);
        const int c = lane & 15, g = lane >> 4;
        const int fvo = (lane & 7) * 128;
        const int w2o = OFF_W2T + (cg * NP + w) * 4096, w2v = lane * 16 + 2048 * (w >> 1);
        const int fsv = tid == 0 ? cg * 128 : OOB;
        u32 fl;
        v4u H[HR][2];
        const int hv0 = (w * 2 + (w & 1)) * 1024 + (g >> 1) * 512 + c * 32 + (g & 1) * 16, hv1 = hv0 ^ 1024;
#define H_LOAD(hf, s)                                                                       \
    {                                                                                       \
        if ((hf) == 0 && 4 * (s) + 4 > NW2_PRE + NW2_A + NW2_B && layer == 0) {             \
            _Pragma("unroll") for (int j = 0; j < 4; ++j)                                   \
                if (4 * (s) + j >= NW2_PRE + NW2_A + NW2_B) W2_LOAD(s, j);                  \
        }                                                                                   \
        H[(s) % HR][0] = LD16S(rws, (s) == 10 && w == 3 ? OOB : hv0, hoff + (hf) * NP * 2048 + (s) * 8192); \
        H[(s) % HR][1] = LD16S(rws, (s) == 10 && w == 3 ? OOB : hv1, hoff + (hf) * NP * 2048 + (s) * 8192); \
    }
#define X2_REQUEST(hf)                                                                                       \
    {                                                                                                        \
        asm volatile("s_waitcnt lgkmcnt(0)" ::: "memory");                                                   \
        glds4(ws + OFF_ST2 + (2 * grp + (hf)) * 4096, lane * 16, (u32)(size_t)smem + L_RED + w * 6144);      \
        glds8(ws + OFF_X2 + grp * 65536 + (hf) * 32768 + w * 8192, lane * 16, ldsb + L_X + (hf) * 32768);    \
    }
#pragma unroll
        for (int hf = 0; hf < 2; ++hf) {
            STAMP(16 + layer * 24 + hf * 5);
            if (layer == 1) {
                v4u ys[8], xs[8];
                LAS char* xp = smem + L_X + hf * 32768 + w * 8192 + lane * 16;
                if (hf == 0) VMW(11); else VMW(14);
                f32x2 pst[8];
#pragma unroll
                for (int i = 0; i < 8; ++i) pst[i] = *(LAS f32x2*)(smem + L_RED + w * 6144 + lane * 8 + i * 512);
#pragma unroll
                for (int i = 0; i < 8; ++i) ys[i] = *(LAS v4u*)(smem + L_Y + w * 8192 + lane * 16 + i * 1024);
                SB();
                f32x2 s2 = ((pst[0] + pst[1]) + (pst[2] + pst[3])) + ((pst[4] + pst[5]) + (pst[6] + pst[7]));
                const float mean = xadd32(s2.x) * (1.f / 512.f);
                const float rs = __builtin_amdgcn_rsqf(fmaxf(xadd32(s2.y) * (1.f / 512.f) - mean * mean, 0.f) + 1e-5f);
                LAS float* st = (LAS float*)(smem + L_MS + w * 256);
                st[lane] = lane < 32 ? rs : -mean * rs;
                const int tok0 = 16 * (((lane >> 1) ^ g) & 1) + 8 * (lane & 1);
                const f32x4 a0 = *(LAS f32x4*)(st + tok0), a1 = *(LAS f32x4*)(st + tok0 + 4);
                const f32x4 b0 = *(LAS f32x4*)(st + 32 + tok0), b1 = *(LAS f32x4*)(st + 36 + tok0);
                if (hf == 0) VMW(3); else VMW(6);
                STAMP(17 + layer * 24 + hf * 5);
#pragma unroll
                for (int i = 0; i < 8; ++i) xs[i] = *(LAS v4u*)(xp + i * 1024);
                SB();
#pragma unroll
                for (int i = 0; i < 8; ++i) {
                    v4u o;
                    f32x2 x;
                    x = up2(xs[i].x); o.x = pk2(fma2(fma2(x, a0.xy, b0.xy), up2(ys[i].x), x));
                    x = up2(xs[i].y); o.y = pk2(fma2(fma2(x, a0.zw, b0.zw), up2(ys[i].y), x));
                    x = up2(xs[i].z); o.z = pk2(fma2(fma2(x, a1.xy, b1.xy), up2(ys[i].z), x));
                    x = up2(xs[i].w); o.w = pk2(fma2(fma2(x, a1.zw, b1.zw), up2(ys[i].w), x));
                    *(LAS v4u*)(xp + i * 1024) = o;
                    if (hf == 0 && i == MIDMIX) {
                        VMW(0);
                        BAR();
                        FLAG(fsv, fgo + 65536 + 32768);
                    }
                    SB();
                }
            }
            v4u WL[8];
#pragma unroll
            for (int n = 0; n < 8; ++n) WL[n] = *(const LAS v4u*)(smem + L_W + w * 8192 + n * 1024 + lane * 16);
            if (hf == 1) VMW(0);
            BAR();
            if (hf == 1) FLAG(fsv, fgo + layer * 131072);
            if (layer + hf == 1) glds8(ws + OFF_Y + grp * 65536 + (1 - hf) * 32768 + w * 8192, lane * 16, ldsb + L_Y);
            STAMP(18 + layer * 24 + hf * 5);
#define W1F(kk, j) ((kk) * 3 + (j) < 8 ? WL[(kk) * 3 + (j) < 8 ? (kk) * 3 + (j) : 0] : W1[(kk) * 3 + (j) < 8 ? 0 : (kk) * 3 + (j) - 8])
            const LAS char* xb[2][2];
#pragma unroll
            for (int i = 0; i < 2; ++i) {
                xb[0][i] = smem + L_X + hf * 32768 + (4 * g + (c >> 2)) * 64 + (((i ^ w ^ g) & 1) << 5) + 8 * (c & 3) + xrot;
                xb[1][i] = xb[0][i] - 2 * xrot;
            }
            f32x4 acc[3][2], accS = {0.f, 0.f, 0.f, 0.f}, accG = {0.f, 0.f, 0.f, 0.f};
#pragma unroll
            for (int j = 0; j < 3; ++j)
#pragma unroll
                for (int i = 0; i < 2; ++i) acc[j][i] = f32x4{0.f, 0.f, 0.f, 0.f};
            v4u X[3][2];
#define G1_LOAD(kk, i)                                                                   \
    {                                                                                    \
        const v2u lo = tr_read(xb[(kk) >> 3][i] + (kk) * 2048), hi = tr_read(xb[(kk) >> 3][i] + (kk) * 2048 + 1024); \
        X[(kk) % 3][i] = v4u{lo.x, lo.y, hi.x, hi.y};                                    \
    }
            G1_LOAD(0, 0) G1_LOAD(0, 1) G1_LOAD(1, 0) G1_LOAD(1, 1)
            SB();
            const v4u ones = {0x3f803f80u, 0x3f803f80u, 0x3f803f80u, 0x3f803f80u};
            LAS f32x2* ls = (LAS f32x2*)(smem + L_ST + hf * 512);
            f32x2 s2[2][2];
            f32x4 ct[3];
            float rs[2], nb[2];
#pragma unroll
            for (int kk = 0; kk < 16; ++kk) {
#pragma unroll
                for (int n = 3 * kk - 8; n < 3 * kk - 5; ++n)
                    if (n >= 0) { if (n < NA1) PIN_A(W1[n < 0 ? 0 : n]); else PIN_V(W1[n < 0 ? 0 : n]); }
                acc[0][0] = mfma(W1F(kk, 0), X[kk % 3][0], acc[0][0]);
                if (kk < 14) G1_LOAD(kk + 2, 0)
                SB();
                acc[1][0] = mfma(W1F(kk, 1), X[kk % 3][0], acc[1][0]);
                if (kk < 14) G1_LOAD(kk + 2, 1)
                SB();
                acc[2][0] = mfma(W1F(kk, 2), X[kk % 3][0], acc[2][0]);
                if (kk < 8) {
                    accS = mfma(ones, X[kk % 3][0], accS);
                    accG = mfma(X[kk % 3][0], X[kk % 3][0], accG);
                }
                if (kk == 9) {
                    const float gd = (c & 2) ? ((c & 1) ? accG[3] : accG[2]) : ((c & 1) ? accG[1] : accG[0]);
                    if (g == (c >> 2)) ls[w * 16 + c] = f32x2{accS[0], gd};
                }
                if (kk == 11) {
#pragma unroll
                    for (int i = 0; i < 2; ++i) {
                        s2[i][0] = ls[((i ^ w) & 1) * 16 + c];
                        s2[i][1] = ls[(((i ^ w) & 1) + 2) * 16 + c];
                    }
#pragma unroll
                    for (int j = 0; j < 3; ++j) ct[j] = *(const LAS f32x4*)(smem + L_CS + (w * 3 + j) * 64 + g * 16);
                }
                if (kk == 13) {
#pragma unroll
                    for (int i = 1; i >= 0; --i) {
                        const f32x2 s = s2[i][0] + s2[i][1];
                        const float mean = s.x * (1.f / 512.f);
                        rs[i] = __builtin_amdgcn_rsqf(fmaxf(s.y * (1.f / 512.f) - mean * mean, 0.f) + 1e-5f);
                        nb[i] = -mean * rs[i];
                    }
                }
                if (hf == 1 && kk == CPOLL) fl = LD4S(fvo, fgo + layer * 131072);
                if (hf == 0 && kk == DPOLL && layer == 1) fl = LD4S(fvo, fgo + 65536 + 32768);
                if (hf == 0 && kk == DCHK && layer == 1) {
                    POLL_FINISH(fl, fvo, fgo + 65536 + 32768);
                    X2_REQUEST(1)
                }
#pragma unroll
                for (int j = 0; j < 3; ++j) acc[j][1] = mfma(W1F(kk, j), X[kk % 3][1], acc[j][1]);
                if (kk == 10) BAR();
                if (kk & 1) {
                    const int N0 = NW2_PRE + (hf ? NW2_A : 0), NN = hf ? NW2_B : NW2_A, PER = (NN + 4) / 5;
                    const int n0 = N0 + (kk >> 1) * PER;
                    if (n0 < N0 + NN && layer == 0) {
#pragma unroll
                        for (int n = n0; n < n0 + PER && n < N0 + NN; ++n) W2_LOAD(n / 4, n % 4);
                    }
                }
                SB();
            }
            STAMP(19 + layer * 24 + hf * 5);
            if (hf == 0 && NW_B < 40 && layer == 0) {
#pragma unroll
                for (int n = NW_B; n < 40; ++n) ct[(n + 8) % 3] = mfma(W1[n], ones, ct[(n + 8) % 3]);
#pragma unroll
                for (int j = 0; j < 3; ++j) *(LAS f32x4*)(smem + L_CS + (w * 3 + j) * 64 + g * 16) = ct[j];
            }
            if (hf == 1) {
                POLL_FINISH(fl, fvo, fgo + layer * 131072);
#pragma unroll
                for (int s = 0; s < NPRE; ++s) H_LOAD(0, s)
                SB();
            }
#pragma unroll
            for (int i = 0; i < 2; ++i)
#pragma unroll
                for (int j = 0; j < 3; ++j) {
                    const int T = T0 + j;
                    const f32x4 u = acc[j][i] * rs[i] + ct[j] * nb[i];
                    const s16x2 z = {0, 0};
                    const v2u o = {__builtin_bit_cast(u32, __builtin_elementwise_max(__builtin_bit_cast(s16x2, pk2(u.xy)), z)),
                                   __builtin_bit_cast(u32, __builtin_elementwise_max(__builtin_bit_cast(s16x2, pk2(u.zw)), z))};
                    ST8S(o, rws, T < Tend ? (((i ^ w) & 1) * 1024 + c * 32 + g * 8) : OOB,
                         hoff + hf * NP * 2048 + (T >> 1) * 2048 + 512 * (T & 1));
                }
            SB();
            STAMP(20 + layer * 24 + hf * 5);
        }
#pragma unroll
        for (int s = NPRE; s < HR; ++s) H_LOAD(0, s)
        SB();
        STAMP(26 + layer * 24);
#pragma unroll
        for (int hf = 0; hf < 2; ++hf) {
            f32x4 acc[2][4];
#pragma unroll
            for (int i = 0; i < 2; ++i)
#pragma unroll
                for (int j = 0; j < 4; ++j) acc[i][j] = f32x4{0.f, 0.f, 0.f, 0.f};
            const int tt = w & 1, chl = 32 * (w >> 1) + 2 * c;
            const int xo = (64 * cg + chl) * 64 + ((tt ^ ((c >> 1) & 1)) << 5) + 8 * g;
            v2u res[2];
#pragma unroll
            for (int s = 0; s < 11; ++s) {
#pragma unroll
                for (int j = 0; j < 4; ++j) PIN_A(W2[s][j]);
#pragma unroll
                for (int i = 0; i < 2; ++i)
#pragma unroll
                    for (int j = 0; j < 4; ++j) acc[i][j] = mfma(H[s % HR][i], W2[s][j], acc[i][j]);
                SB();
                if (s == (hf == 0 ? NPRE : 4)) {
                    BAR();
                    if (hf == 0) FLAG(fsv, fgo + layer * 131072 + 32768);
                    else FLAG(layer == 0 ? fsv : OOB, fgo + 65536);
                }
                if (s + HR < 11) H_LOAD(hf, s + HR)
                if (s == CPOLL2 && hf == 0) fl = LD4S(fvo, fgo + layer * 131072 + 32768);
                if (s == 8) {
                    res[0] = *(const LAS v2u*)(smem + L_X + hf * 32768 + xo);
                    res[1] = *(const LAS v2u*)(smem + L_X + hf * 32768 + xo + 64);
                }
                if (s == 10 && hf == 1 && layer == 0) fl = LD4S(fvo, fgo + 65536);
                SB();
            }
            STAMP(27 + layer * 24 + hf * 4);
            if (hf == 0) {
                POLL_FINISH(fl, fvo, fgo + layer * 131072 + 32768);
#pragma unroll
                for (int s = 0; s < 4; ++s) H_LOAD(1, s)
                SB();
            }
            STAMP(28 + layer * 24 + hf * 4);
            LAS f32x4* red = (LAS f32x4*)(smem + L_RED);
#pragma unroll
            for (int i = 0; i < 2; ++i)
#pragma unroll
                for (int j = 0; j < 4; ++j) {
                    const int src = i + 2 * (j >> 1);
                    if (src) red[(((w ^ src) * 3 + src - 1) * 2 + (j & 1)) * 64 + lane] = acc[i][j];
                }
            BAR();
            f32x4 rd[6];
#pragma unroll
            for (int i = 0; i < 6; ++i) rd[i] = red[(w * 6 + i) * 64 + lane];
            SB();
            if (hf == 1 && layer == 0) {
                POLL_FINISH(fl, fvo, fgo + 65536);
                X2_REQUEST(0)
            }
            STAMP(29 + layer * 24 + hf * 4);
            f32x2 xf[2][2];
#pragma unroll
            for (int j = 0; j < 2; ++j) {
                const f32x4 t = (acc[0][j] + rd[j]) + (rd[2 + j] + rd[4 + j]);
                xf[j][0] = up2(res[j].x) + t.xy;
                xf[j][1] = up2(res[j].y) + t.zw;
            }
            if (layer == 1) {
#pragma unroll
                for (int r = 0; r < 4; ++r)
                    __builtin_amdgcn_raw_buffer_store_b32(pk2bf(xf[0][r >> 1][r & 1], xf[1][r >> 1][r & 1]), mkrs(out, 2 * MB),
                                                          (16 * tt + 4 * g + r) * 1024 + chl * 2, (64 * grp + 32 * hf) * 1024 + cg * 128, 0);
            } else {
                u32 xq[2][2];
#pragma unroll
                for (int j = 0; j < 2; ++j) {
                    xq[j][0] = pk2(xf[j][0]);
                    xq[j][1] = pk2(xf[j][1]);
                    ST8S((v2u{xq[j][0], xq[j][1]}), rws, xo + 64 * j, OFF_X2 + (2 * grp + hf) * 32768);
                }
                f32x2 sm[2], sq[2];
#pragma unroll
                for (int r = 0; r < 2; ++r) {
                    const f32x2 x0 = up2(xq[0][r]), x1 = up2(xq[1][r]);
                    sm[r] = x0 + x1;
                    sq[r] = fma2(x0, x0, x1 * x1);
                }
                f32x4 s4;
                {
                    const bool odd = c & 1;
                    const f32x4 keep = odd ? f32x4{sm[1].x, sq[1].x, sm[1].y, sq[1].y} : f32x4{sm[0].x, sq[0].x, sm[0].y, sq[0].y};
                    const f32x4 give = odd ? f32x4{sm[0].x, sq[0].x, sm[0].y, sq[0].y} : f32x4{sm[1].x, sq[1].x, sm[1].y, sq[1].y};
#pragma unroll
                    for (int e = 0; e < 4; ++e) {
                        const float gv = give[e];
                        float t = keep[e] + __builtin_bit_cast(float, __builtin_amdgcn_update_dpp(0, __builtin_bit_cast(int, gv), 0xb1, 0xf, 0xf, true));
                        t = dppadd<0x122>(t);
                        t = dppadd<0x124>(t);
                        t = dppadd<0x128>(t);
                        s4[e] = t;
                    }
                }
                ST16S(__builtin_bit_cast(v4u, s4), rws, c < 2 ? (cg * 2 + (w >> 1)) * 256 + (16 * tt + 4 * g + 2 * c) * 8 : OOB,
                      OFF_ST2 + (2 * grp + hf) * 4096);
            }
            SB();
            if (hf == 0) {
#pragma unroll
                for (int s = 4; s < HR; ++s) H_LOAD(1, s)
                SB();
            }
            STAMP(30 + layer * 24 + hf * 4);
        }
    }
    asm volatile(".Lpf_end:");
}

extern "C" void kernel_launch(void* const* d_in, const int* in_sizes, int n_in, void* d_out, int out_size, void* d_ws,
                              size_t ws_size, hipStream_t stream) {
    constexpr int lds = L_BYTES;
    (void)hipFuncSetAttribute((const void*)k_all, hipFuncAttributeMaxDynamicSharedMemorySize, lds);
    k_all<<<256, 256, lds, stream>>>((const u16*)d_in[0], (const u16*)d_in[1], (const u16*)d_in[2], (const u16*)d_in[3],
                                     (const u16*)d_in[4], (const u16*)d_in[5], (const u16*)d_in[7], (const u16*)d_in[8],
                                     (u16*)d_out, (char*)d_ws);
}
```

```cpp
#include <hip/hip_runtime.h>
#include <hip/hip_bf16.h>
#include <stdint.h>

typedef unsigned short u16;
typedef unsigned int u32;
typedef unsigned long long u64;
typedef u32 v4u __attribute__((ext_vector_type(4)));
typedef u32 v2u __attribute__((ext_vector_type(2)));
typedef float f32x4 __attribute__((ext_vector_type(4)));
typedef float f32x2 __attribute__((ext_vector_type(2)));
typedef short bf16x8 __attribute__((ext_vector_type(8)));
typedef short s16x4 __attribute__((ext_vector_type(4)));
typedef short s16x2 __attribute__((ext_vector_type(2)));
typedef __bf16 bf16x2 __attribute__((ext_vector_type(2)));
#define LAS __attribute__((address_space(3)))
#define GAS __attribute__((address_space(1)))
#define DEV __device__ __forceinline__
#define RLX __ATOMIC_RELAXED, __HIP_MEMORY_SCOPE_AGENT
typedef __amdgpu_buffer_rsrc_t rsrc_t;

constexpr int S_ = 2048, D_ = 512, F_ = 1376;
constexpr int NP = 43;
constexpr int NT = 86;
constexpr int MB = 1 << 20;
constexpr int OFF_FL = 0, OFF_ST0 = 512 * 1024;
constexpr int OFF_UR = 1 * MB, OFF_X0T = 3 * MB, OFF_Y = 5 * MB, OFF_X1 = 7 * MB, OFF_W1T = 11 * MB,
              OFF_W2T = 13 * MB, OFF_H = 15 * MB, OFF_X2 = OFF_H + 64 * NP * 2048, OFF_ST2 = OFF_X2 + 2 * MB, WS_USED = 27 * MB;
static_assert(OFF_ST2 + 64 * 4096 - OFF_H <= 256 * 31 * 1024, "the marked region is 31 KiB per workgroup");
constexpr int FL_EPOCH = 0, FL_A = 4096, FL_A2 = 40960, FL_B = 49152, FL_W = 57344, FL_G = 81920;
constexpr int SC1 = 16, OOB = 0x40000000;
constexpr int L_X = 0, L_Y = 65536, L_W = 98304, L_RED = 131072, L_ST = 155648, L_MS = L_ST + 2560, L_CS = L_MS + 1024, L_BYTES = L_CS + 1024;
constexpr int CV_E = 0, CV_O = 4160, CV_U = 8320, CV_CH = 12928, CV_X = 2 * CV_CH, L_P0 = 49152;
constexpr u32 SPIN_CAP = 1u << 18;
#define NA1 20
#define HR 6
#define PSLEEP 2
#define SECMODE 0
#define QAUX 2
#define NTAUX 2
#define WAUX 2
#define A2R 1
#define A1DELAY 12
#define WPOS 2
#define NW_B 34
#define NW2_A 9
#define NW2_B 27
#define NPRE 2
#define CPOLL 10
#define CPOLL2 8
#define DPOLL 5
#define DCHK 10
#define MIDMIX 3
#define CODEPF 1
#define QEARLY 1
#define SECDELAY 1
#define PUBI -1
#define POLI 4
#define EARLYDELAY 0
#define WSTEPS 9
#define NPRE2 4
#define DPOLL2 10
#define ENDINV 0
#define CLRPOS 4
#define NW_CONV 8
#define NW2_PRE 0

static_assert(NW_CONV <= (3 - A2R) * 18 - 12 && NW_CONV < NW_B && NW_B <= 40, "slots");
template <int N> struct IC { static constexpr int value = N; };
DEV float lo2f(u32 d) { return __builtin_bit_cast(float, d << 16); }
DEV float hi2f(u32 d) { return __builtin_bit_cast(float, d & 0xffff0000u); }
DEV f32x2 up2(u32 d) { return f32x2{lo2f(d), hi2f(d)}; }
DEV u32 pk2(f32x2 x) { return __builtin_bit_cast(u32, __builtin_convertvector(x, bf16x2)); }
DEV u32 pk2bf(float a, float b) { return pk2(f32x2{a, b}); }
DEV f32x2 fma2(f32x2 a, f32x2 b, f32x2 c) { return __builtin_elementwise_fma(a, b, c); }
DEV f32x4 mfma(v4u a, v4u b, f32x4 c) {
    return __builtin_amdgcn_mfma_f32_16x16x32_bf16(__builtin_bit_cast(bf16x8, a), __builtin_bit_cast(bf16x8, b), c, 0, 0, 0);
}
DEV v2u tr_read(const LAS char* p) {
    return __builtin_bit_cast(v2u, __builtin_amdgcn_ds_read_tr16_b64_v4i16((LAS s16x4*)p));
}
DEV float xadd16(float v) {
    auto r = __builtin_amdgcn_permlane16_swap(__builtin_bit_cast(u32, v), __builtin_bit_cast(u32, v), false, false);
    const u32 r0 = r[0], r1 = r[1];
    return __builtin_bit_cast(float, r0) + __builtin_bit_cast(float, r1);
}
DEV float xadd32(float v) {
    auto r = __builtin_amdgcn_permlane32_swap(__builtin_bit_cast(u32, v), __builtin_bit_cast(u32, v), false, false);
    const u32 r0 = r[0], r1 = r[1];
    return __builtin_bit_cast(float, r0) + __builtin_bit_cast(float, r1);
}
template <int CTRL> DEV float dppadd(float v) {
    return v + __builtin_bit_cast(float, __builtin_amdgcn_update_dpp(0, __builtin_bit_cast(int, v), CTRL, 0xf, 0xf, true));
}
DEV float row16sum(float v) {
    v = dppadd<0x121>(v);
    v = dppadd<0x122>(v);
    v = dppadd<0x124>(v);
    v = dppadd<0x128>(v);
    return v;
}
typedef unsigned short u16x2 __attribute__((ext_vector_type(2)));
template <int NEG> DEV u32 relu2(u32 v) {
    if (NEG) return __builtin_bit_cast(u32, __builtin_elementwise_max(__builtin_bit_cast(u16x2, v), u16x2{0x8000, 0x8000}));
    return __builtin_bit_cast(u32, __builtin_elementwise_max(__builtin_bit_cast(s16x2, v), s16x2{0, 0}));
}
DEV rsrc_t mkrs(const void* p, int bytes) { return __builtin_amdgcn_make_buffer_rsrc((void*)p, 0, bytes, 0x00020000); }
#define LD16(r, vo, so) __builtin_amdgcn_raw_buffer_load_b128(r, vo, so, QAUX)
#define LD16N(r, vo, so) __builtin_amdgcn_raw_buffer_load_b128(r, vo, so, NTAUX)
#define LD4(r, vo, so) __builtin_amdgcn_raw_buffer_load_b32(r, vo, so, WAUX)
#define LD16S(r, vo, so) __builtin_amdgcn_raw_buffer_load_b128(r, vo, so, SC1)
#define LD8S(r, vo, so) __builtin_amdgcn_raw_buffer_load_b64(r, vo, so, SC1)
#define ST16S(v, r, vo, so) __builtin_amdgcn_raw_buffer_store_b128(v, r, (vo) + (so), 0, SC1)
#define ST8S(v, r, vo, so) __builtin_amdgcn_raw_buffer_store_b64(v, r, vo, so, SC1)
#define SB() __builtin_amdgcn_sched_barrier(0)
#define MF_(acc, ca, a, cb, b) asm volatile("v_mfma_f32_16x16x32_bf16 %0, %1, %2, %0" : "+v"(acc) : ca(a), cb(b))
#define MF0_(acc, ca, a, cb, b) asm volatile("v_mfma_f32_16x16x32_bf16 %0, %1, %2, 0" : "=&v"(acc) : ca(a), cb(b))
#define MF(first, acc, ca, a, cb, b) do { if (first) MF0_(acc, ca, a, cb, b); else MF_(acc, ca, a, cb, b); } while (0)
#define MF_DONE() asm volatile("s_nop 7" ::: "memory")
#define PIN_A(x) asm volatile("" : "+a"(x))
#define PIN_V(x) asm volatile("" : "+v"(x))
#define BAR() do { SB(); asm volatile("s_waitcnt lgkmcnt(0)\n\ts_barrier" ::: "memory"); SB(); } while (0)
#define VMW(n) do { SB(); asm volatile("s_waitcnt vmcnt(%0)" ::"n"(n) : "memory"); SB(); } while (0)
DEV void glds8(const char* sbase, u32 voff, u32 lds) {
    u32 keep;
    asm volatile(
        "s_mov_b32 %0, m0\n\ts_mov_b32 m0, %3\n\ts_nop 0\n\t"
        "global_load_lds_dwordx4 %1, %2 offset:-4096 sc1\n\t"
        "global_load_lds_dwordx4 %1, %2 offset:-3072 sc1\n\t"
        "global_load_lds_dwordx4 %1, %2 offset:-2048 sc1\n\t"
        "global_load_lds_dwordx4 %1, %2 offset:-1024 sc1\n\t"
        "global_load_lds_dwordx4 %1, %2 sc1\n\t"
        "global_load_lds_dwordx4 %1, %2 offset:1024 sc1\n\t"
        "global_load_lds_dwordx4 %1, %2 offset:2048 sc1\n\t"
        "global_load_lds_dwordx4 %1, %2 offset:3072 sc1\n\t"
        "s_mov_b32 m0, %0"
        : "=&s"(keep) : "v"(voff), "s"(sbase + 4096), "s"(lds + 4096) : "memory");
}
DEV void glds4(const char* sbase, u32 voff, u32 lds) {
    u32 keep;
    asm volatile(
        "s_mov_b32 %0, m0\n\ts_mov_b32 m0, %3\n\ts_nop 0\n\t"
        "global_load_lds_dwordx4 %1, %2 sc1\n\t"
        "global_load_lds_dwordx4 %1, %2 offset:1024 sc1\n\t"
        "global_load_lds_dwordx4 %1, %2 offset:2048 sc1\n\t"
        "global_load_lds_dwordx4 %1, %2 offset:3072 sc1\n\t"
        "s_mov_b32 m0, %0"
        : "=&s"(keep) : "v"(voff), "s"(sbase), "s"(lds) : "memory");
}
template <int OFF> DEV void glds1(const char* sbase, u32 voff, u32 lds) {
    u32 keep;
    asm volatile("s_mov_b32 %0, m0\n\ts_mov_b32 m0, %3\n\ts_nop 0\n\tglobal_load_lds_dwordx4 %1, %2 offset:%4 sc1\n\ts_mov_b32 m0, %0"
                 : "=&s"(keep) : "v"(voff), "s"(sbase), "s"(lds), "n"(OFF) : "memory");
}
DEV void glds1x(int i, const char* sbase, u32 voff, u32 lds) {
    switch (i) {
        case 0: glds1<-4096>(sbase, voff, lds); break;
        case 1: glds1<-3072>(sbase, voff, lds); break;
        case 2: glds1<-2048>(sbase, voff, lds); break;
        case 3: glds1<-1024>(sbase, voff, lds); break;
        case 4: glds1<0>(sbase, voff, lds); break;
        case 5: glds1<1024>(sbase, voff, lds); break;
        case 6: glds1<2048>(sbase, voff, lds); break;
        default: glds1<3072>(sbase, voff, lds); break;
    }
}
DEV u32 mktag(u32 e) { return (u32)__builtin_amdgcn_readfirstlane(e) * 1664525u + 1013904223u; }
#define LD4S(vo, so) __builtin_amdgcn_raw_buffer_load_b32(rws, vo, so, SC1)
#define FLAG(vo, so) __builtin_amdgcn_raw_buffer_store_b32(tag, rws, vo, so, SC1)
#define POLL_FINISH(f, vo, so)                                            \
    do {                                                                  \
        if (!__all(f == tag)) {                                           \
            u32 spins_ = 0;                                               \
            bool ok_ = f == tag;                                          \
            while (!dead) {                                               \
                __builtin_amdgcn_s_sleep(PSLEEP);                         \
                asm volatile("" ::: "memory");                            \
                const u32 g_ = LD4S(ok_ ? OOB : (vo), so);                \
                ok_ = ok_ || g_ == tag;                                   \
                if (__all(ok_)) break;                                    \
                if (++spins_ > SPIN_CAP) dead = true;                     \
            }                                                             \
        }                                                                 \
        __builtin_amdgcn_fence(__ATOMIC_ACQUIRE, "wavefront");            \
        SB();                                                             \
    } while (0)
#define ALLOK(P) __all(P.x == tag && P.y == tag && P.z == tag && P.w == tag)
#define POLL_ALL(f, so)                                                   \
    do {                                                                  \
        if (!ALLOK(f)) {                                                  \
            u32 spins_ = 0;                                               \
            while (!dead) {                                               \
                __builtin_amdgcn_s_sleep(8);                              \
                asm volatile("" ::: "memory");                            \
                const v4u g_ = LD16S(rws, lane * 16, so);                 \
                if (ALLOK(g_)) break;                                     \
                if (++spins_ > SPIN_CAP) dead = true;                     \
            }                                                             \
        }                                                                 \
        __builtin_amdgcn_fence(__ATOMIC_ACQUIRE, "wavefront");            \
        SB();                                                             \
    } while (0)
#define STAMP(i)

__global__ __launch_bounds__(256, 1) void k_all(const u16* __restrict__ q, const u16* __restrict__ k, const u16* __restrict__ v,
                                                const u16* __restrict__ sk, const u16* __restrict__ ssmk,
                                                const u16* __restrict__ Dv, const u16* __restrict__ w1,
                                                const u16* __restrict__ w2, u16* __restrict__ out, char* __restrict__ ws) {
    extern __shared__ __attribute__((aligned(16))) char smem_[];
    LAS char* smem = (LAS char*)smem_;
    const int b = blockIdx.x, tid = threadIdx.x, w = __builtin_amdgcn_readfirstlane(tid >> 6);
    const int xc = b & 7, jb = b >> 3;
    const rsrc_t rws = mkrs(ws, WS_USED);
    bool dead = false;
    STAMP(0);
    const int ch0 = 64 * xc + 2 * jb;
    const int grp = 4 * xc + (b >> 6), cg = jb & 7;
    const int T0 = 11 * cg + 3 * w, Tend = cg == 7 ? NT : 11 * cg + 11;
    u32 tag;
    v4u W1[40], W2[11][4];
    {
        const int lane = tid & 63, c = lane & 15, g = lane >> 4;
#define Q_LOADS()                                                                                              \
    _Pragma("unroll") for (int i = 0; i < 4; ++i) hq[i] = LD16(r2, (w < 2 ? lane * 16 : qvo) + i * 1024, 0);   \
    _Pragma("unroll") for (int i = 0; i < 4; ++i) xr[i] = LD16(r2, (8 * b + 4 * half + 4 + i) * 1024 + lane * 16 - (w < 2 ? 0 : 4096), 0);
#define W_LOADS()                                                                                              \
    _Pragma("unroll") for (int i = 0; i < 8; ++i)                                                              \
        wr[i] = __builtin_amdgcn_raw_buffer_load_b128(                                                         \
            rw, wvo, is1 ? (16 * (i & 1) + 4 * (i >> 1)) * F_ * 2 : (16 * ((i >> 1) & 1) + (i >> 2) + 8 * (i & 1)) * D_ * 2, WAUX);
#define CLR_OFF(i) ((i) < 22 ? OFF_H + grp * 176128 + cg * 22016 + (i) * 1024 : (i) < 30 ? OFF_X2 + grp * 65536 + cg * 8192 + ((i) - 22) * 1024 \
                    : (i) == 30 ? OFF_ST2 + grp * 8192 + cg * 1024 : OOB)
        u32 pf;
        {
            u64 pc;
            u32 len;
            asm volatile("s_getpc_b64 %0\n.Lpf_base:\n\ts_mov_b32 %1, .Lpf_end-.Lpf_base" : "=s"(pc), "=s"(len));
            pf = __builtin_amdgcn_raw_buffer_load_b32(mkrs((const void*)pc, len), w == 3 && lane < 14 ? jb * 1792 + lane * 128 : OOB, 0, 0);
        }
        const int cw = w >> 1, half = w & 1, ch = ch0 + cw;
        u32 draw;
        {
            const int row = 2 * (tid >> 3), s0 = 64 * jb + row;
            const int vo = s0 * 1024 + xc * 128 + (lane & 7) * 16;
            const rsrc_t rk = mkrs(k, 2 * MB), rv = mkrs(v, 2 * MB);
            v4u kk[5], vv[2], sw[4];
#pragma unroll
            for (int t = 0; t < 5; ++t) kk[t] = LD16N(rk, s0 + t - 3 >= 0 ? vo + (t - 3) * 1024 : OOB, 0);
#pragma unroll
            for (int t = 0; t < 2; ++t) vv[t] = LD16N(rv, vo + t * 1024, 0);
#pragma unroll
            for (int t = 0; t < 4; ++t) sw[t] = *(const v4u*)(sk + (64 * xc + 8 * (lane & 7)) * 4 + 8 * t);
            const u32 epoch = LD4S(0, OFF_FL + FL_EPOCH);
            draw = __builtin_amdgcn_raw_buffer_load_b16(mkrs(Dv, 1024), 0, ch * 2, 0);
            const rsrc_t r2 = mkrs(w < 2 ? ssmk + (ch0 + half) * S_ : q, w < 2 ? 4096 : 2 * MB);
            const int qvo = (64 * jb + 32 * half + 4 * (lane >> 3)) * 1024 + xc * 128 + (lane & 7) * 16;
            v4u hq[4], xr[4], wr[8];
            const int wu = 2 * b + half;
            const bool is1 = wu < 176;
            const int u11 = (wu * 5958) >> 16, ua = is1 ? u11 : (wu - 176) >> 2, ub = is1 ? wu - 11 * u11 : wu & 3;
            const int rowb = is1 ? F_ * 2 : D_ * 2;
            const rsrc_t rw = mkrs(is1 ? w1 : w2, wu < 348 && w >= 2 ? F_ * D_ * 2 : 0);
            const int wvo = (32 * ua + (g << (is1 ? 0 : 1))) * rowb + ub * 256 + c * 16;
            Q_LOADS()
            W_LOADS()
            int wdst;
            {
                const int T = 8 * ub + lane, tc = (T * 5958) >> 16, tr = T - 11 * tc, tw = (tr * 22) >> 6, tj = tr - 3 * tw;
                const int o1 = OFF_W1T + (tc * 4 + tw) * 49152 + (((ua - 4 * tw) & 15) * 3 + tj) * 1024;
                const int o2 = OFF_W2T + ((2 * ub + (lane >> 2)) * NP + ua) * 4096 + (lane & 3) * 1024;
                wdst = T < (is1 ? NT : wu < 348 ? 1024 : 0) ? (is1 ? o1 : o2) : OOB;
                PIN_V(wdst);
            }
            SB();
#define W_STORES()                                                                                          \
    {                                                                                                       \
        LAS char* wl = smem + L_Y + half * 16384;                                                           \
        _Pragma("unroll") for (int i = 0; i < 8; ++i) *(LAS v4u*)(wl + i * 1024 + (i >> 1) * 64 + lane * 16) = wr[i]; \
        _Pragma("unroll") for (int blk = 0; blk < 4; ++blk) {                                               \
            u32 d[8];                                                                                       \
            _Pragma("unroll") for (int e = 0; e < 8; ++e) d[e] = *(const LAS u32*)(wl + g * 2112 + c * 4 + e * 256 + blk * 64); \
            _Pragma("unroll") for (int u = 0; u < 2; ++u) {                                                 \
                v4u fr;                                                                                     \
                _Pragma("unroll") for (int e = 0; e < 4; ++e)                                               \
                    fr[e] = __builtin_amdgcn_perm(d[2 * e + 1], d[2 * e], u ? 0x07060302u : 0x05040100u);  \
                ST16S(fr, rws, lane * 16, __builtin_amdgcn_readlane(wdst, 2 * blk + u));                                            \
            }                                                                                               \
        }                                                                                                   \
    }
#pragma unroll
            for (int t = 0; t < 2; ++t) {
                v4u o;
#pragma unroll
                for (int j = 0; j < 4; ++j) {
                    f32x2 a = up2(kk[t][j]) * f32x2{hi2f(sw[j].y), hi2f(sw[j].w)};
                    a = fma2(up2(kk[t + 1][j]), f32x2{lo2f(sw[j].y), lo2f(sw[j].w)}, a);
                    a = fma2(up2(kk[t + 2][j]), f32x2{hi2f(sw[j].x), hi2f(sw[j].z)}, a);
                    a = fma2(up2(kk[t + 3][j]), f32x2{lo2f(sw[j].x), lo2f(sw[j].z)}, a);
                    o[j] = pk2(a * up2(vv[t][j]));
                }
                *(LAS v4u*)(smem + L_P0 + (row + t) * 128 + (lane & 7) * 16) = o;
            }
            tag = mktag(epoch);
            PIN_V(draw);
            PIN_V(pf);
            BAR();
            STAMP(1);
            const int qd = c >> 2;
            if (w < 2) {
                const LAS char* base = smem + L_P0 + 64 * half + 8 * (c & 3) + (63 - 8 * g - qd) * 128;
                const int dst = (64 * xc + 32 * half + c) * 4096 + 16 * g + OFF_UR + (1984 - 64 * jb) * 2;
                v2u a[8];
#pragma unroll
                for (int i = 0; i < 8; ++i) a[i] = tr_read(base + (i >> 2) * 32 - (32 * ((i >> 1) & 1) + 4 * (i & 1)) * 128);
#pragma unroll
                for (int i = 0; i < 4; ++i)
                    ST16S((v4u{a[2 * i].x, a[2 * i].y, a[2 * i + 1].x, a[2 * i + 1].y}), rws, dst, (i & 1) * 64 + (i >> 1) * 65536);
                SB();
                LAS char* cv = smem + half * CV_CH;
                const v4u zero = {0, 0, 0, 0};
                if (lane < 33) *(LAS v4u*)(cv + (lane < 30 ? CV_U + 4096 + lane * 16 : lane < 32 ? CV_E + 16 * (lane - 30) : CV_O)) = zero;
                if (lane == 0) *(LAS v4u*)(cv + CV_O + 16) = v4u{0, 0, 0, hq[0].x << 16};
#pragma unroll
                for (int i = 0; i < 4; ++i) {
                    const u32 nx = i < 3 ? __builtin_amdgcn_readlane(hq[i < 3 ? i + 1 : i].x, 0) : 0u;
                    v4u o;
                    o.x = __builtin_amdgcn_alignbyte(hq[i].y, hq[i].x, 2);
                    o.y = __builtin_amdgcn_alignbyte(hq[i].z, hq[i].y, 2);
                    o.z = __builtin_amdgcn_alignbyte(hq[i].w, hq[i].z, 2);
                    o.w = __builtin_amdgcn_alignbyte((u32)__builtin_amdgcn_update_dpp((int)nx, (int)hq[i].x, 0x130, 0xf, 0xf, false), hq[i].w, 2);
                    *(LAS v4u*)(cv + CV_E + 32 + lane * 16 + i * 1024) = hq[i];
                    *(LAS v4u*)(cv + CV_O + 32 + lane * 16 + i * 1024) = o;
                }
                VMW(0);
#pragma unroll
                for (int i = 0; i < 4; ++i) PIN_V(xr[i]);
#pragma unroll
                for (int i = 0; i < 8; ++i) PIN_V(wr[i]);
                STAMP(2);
                volatile LAS u32* hs = (volatile LAS u32*)(smem + L_ST);
                hs[half] = tag;
                FLAG(hs[half ^ 1] == tag && lane == 0 ? (xc * 32 + jb) * 128 : OOB, OFF_FL + FL_A);
                if (w == 0) {
                    const int fo = OFF_FL + FL_A + xc * 4096, fv = lane < 32 ? lane * 128 : OOB;
                    __builtin_amdgcn_s_sleep(A1DELAY);
                    u32 f = LD4S(fv, fo);
                    if (lane >= 32) f = tag;
                    POLL_FINISH(f, fv, fo);
                    STAMP(5);
                    glds4(ws + OFF_UR + ch0 * 4096, lane * 16, (u32)(size_t)(smem + CV_U));
                    glds4(ws + OFF_UR + ch0 * 4096 + 4096, lane * 16, (u32)(size_t)(smem + CV_CH + CV_U));
                }
            } else {
                __builtin_amdgcn_s_sleep(SECDELAY);
                SB();
                LAS char* img = smem + L_P0 + 8192 + half * 4096;
#pragma unroll
                for (int t = 0; t < 4; ++t) *(LAS v4u*)(img + (4 * (lane >> 3) + t) * 128 + (lane & 7) * 16) = hq[t];
                v2u a[8];
#pragma unroll
                for (int i = 0; i < 8; ++i) a[i] = tr_read(img + 8 * (c & 3) + (i >> 1) * 32 + (4 * (i & 1) + 8 * g + qd) * 128);
#pragma unroll
                for (int i = 0; i < 4; ++i)
                    ST16S((v4u{a[2 * i].x, a[2 * i].y, a[2 * i + 1].x, a[2 * i + 1].y}), rws, (64 * xc + c) * 4096 + 16 * g,
                          OFF_X0T + 128 * jb + 64 * half + i * 65536);
                SB();
                f32x4 sta, stb;
#pragma unroll
                for (int i = 0; i < 4; ++i) {
                    f32x2 x[4];
#pragma unroll
                    for (int j = 0; j < 4; ++j) x[j] = up2(xr[i][j]);
                    const f32x2 s2v = (x[0] + x[1]) + (x[2] + x[3]);
                    const f32x2 q2v = fma2(x[0], x[0], x[1] * x[1]) + fma2(x[2], x[2], x[3] * x[3]);
                    sta[i] = s2v.x + s2v.y;
                    stb[i] = q2v.x + q2v.y;
                }
#pragma unroll
                for (int i = 0; i < 4; ++i) { sta[i] = row16sum(sta[i]); stb[i] = row16sum(stb[i]); }
#pragma unroll
                for (int i = 0; i < 4; ++i) {
                    const float mean = xadd32(xadd16(sta[i])) * (1.f / 512.f);
                    const float rs = __builtin_amdgcn_rsqf(fmaxf(xadd32(xadd16(stb[i])) * (1.f / 512.f) - mean * mean, 0.f) + 1e-5f);
                    sta[i] = rs;
                    stb[i] = -mean * rs;
                }
                ST16S(__builtin_bit_cast(v4u, lane ? stb : sta), rws, lane < 2 ? (8 * b + 4 * half) * 4 + lane * 8192 : OOB, OFF_ST0);
                SB();
                W_STORES()
                STAMP(3);
#define A2_PUBLISH()                                                                              \
    {                                                                                             \
        VMW(0);                                                                                   \
        volatile LAS u32* hs = (volatile LAS u32*)(smem + L_ST + 8);                              \
        hs[half] = tag;                                                                           \
        FLAG(hs[half ^ 1] == tag && lane < 8 ? lane * 1024 + b * 4 : OOB, OFF_FL + FL_A2);        \
    }
                A2_PUBLISH()
                STAMP(4);
            }
        }
        const LAS char* ab = smem + cw * CV_CH + ((c & 1) ? CV_E + 2 * (1 + c) : CV_O + 2 * c) + 16 * g + 256 * half;
        const LAS char* bb = smem + cw * CV_CH + CV_U + 4064 - 3584 - 32 * c + 16 * g + 256 * half;
        v4u A[2][8], B[2][8];
#define CV_LA(buf, r, i) { const LAS u32* pa = (const LAS u32*)(ab + 512 * (i) + 64 * (r)); A[buf][i] = v4u{pa[0], pa[1], pa[2], pa[3]}; }
#define CV_LB(buf, r, i) B[buf][i] = *(const LAS v4u*)(bb + 512 * (7 - (i)) + 64 * (r));
        if (w == 0) VMW(0);
        BAR();
        STAMP(6);
        v4u fa;
        CV_LA(0, 0, 0)
#pragma unroll
        for (int i = 0; i < 8; ++i) CV_LB(0, 0, i)
#pragma unroll
        for (int i = 1; i < 8; ++i) CV_LA(0, 0, i)
        f32x4 acc[8];
        v2u x0[4], uu[4];
        f32x4 sa[4], sb[4];
        const int w1o = OFF_W1T + (cg * 4 + w) * 49152;
        int w1v[3];
#pragma unroll
        for (int j = 0; j < 3; ++j) w1v[j] = T0 + j < Tend ? j * 1024 + lane * 16 : OOB;
        const int tk = 1024 * half + 16 * c + 4 * g;
#define W1_LOAD(n) W1[n] = LD16S(rws, w1v[((n) + 8) % 3], w1o + (((n) + 8) / 3) * 3072)
        auto conv_step = [&](auto r_) __attribute__((always_inline)) {
            constexpr int r = decltype(r_)::value;
            int idx = 0;
#pragma unroll
            for (int al = 0; al < 8; ++al)
#pragma unroll
                for (int be = 0; be + al < 8; ++be) {
                    MF(r == 0 && al == 0, acc[al + be], "v", A[r & 1][al], "v", B[r & 1][be]);
                    if (r < 3 && idx >= 4 && idx < 20) {
                        if (idx & 1) CV_LA((r + 1) & 1, r + 1, (idx - 4) >> 1) else CV_LB((r + 1) & 1, r + 1, (idx - 4) >> 1)
                    }
                    if (r == 3 && idx < 4) uu[idx] = *(const LAS v2u*)(smem + cw * CV_CH + CV_U + 2 * (2044 - tk) - 512 * idx);
                    if (r == CLRPOS - 1 && idx % 4 == 0 && idx < 32)
                        ST16S((v4u{~0u, ~0u, ~0u, ~0u}), rws, lane * 16, CLR_OFF(idx / 4 * 4 + w));
                    if (r * 36 + idx == PUBI && w >= 2) { A2_PUBLISH() STAMP(4); }
                    if (r * 36 + idx == POLI) fa = LD16S(rws, lane * 16, OFF_FL + FL_A2 + xc * 1024);
                    if (r > A2R && idx % 2 == 1) {
                        const int n = (r - A2R - 1) * 18 + idx / 2;
                        if (n < 4) x0[n] = LD8S(rws, tk * 2 + n * 512, OFF_X0T + ch * 4096);
                        else if (n < 8) sa[n - 4] = __builtin_bit_cast(f32x4, LD16S(rws, tk * 4 + (n - 4) * 1024, OFF_ST0));
                        else if (n < 12) sb[n - 8] = __builtin_bit_cast(f32x4, LD16S(rws, tk * 4 + (n - 8) * 1024, OFF_ST0 + 8192));
                        else if (n - 12 < NW_CONV) W1_LOAD(n - 12);
                    }
                    SB();
                    ++idx;
                }
            if (r == A2R) {
                POLL_ALL(fa, OFF_FL + FL_A2 + xc * 1024);
                STAMP(7);
            }
        };
        conv_step(IC<0>{});
        conv_step(IC<1>{});
        conv_step(IC<2>{});
        conv_step(IC<3>{});
        MF_DONE();
#pragma unroll
        for (int i = 0; i < 8; ++i) PIN_V(acc[i]);
        STAMP(8);
        LAS f32x4* xch = (LAS f32x4*)(smem + CV_X);
        f32x4 fin[4];
        if (half) {
#pragma unroll
            for (int i = 0; i < 4; ++i) { xch[(w * 4 + i) * 64 + lane] = acc[i]; fin[i] = acc[4 + i]; }
        } else {
#pragma unroll
            for (int i = 0; i < 4; ++i) { xch[(w * 4 + i) * 64 + lane] = acc[4 + i]; fin[i] = acc[i]; }
        }
        BAR();
        {
            f32x4 oth[4];
#pragma unroll
            for (int i = 0; i < 4; ++i) oth[i] = xch[((w ^ 1) * 4 + i) * 64 + lane];
            SB();
            const float dval = lo2f(draw);
            const int off = (32 * half + (c >> 1)) * 32768 + ch * 64 + (((c & 1) ^ ((ch >> 2) & 1)) << 5) + 8 * g;
#pragma unroll
            for (int i = 0; i < 4; ++i) {
                const f32x4 y = fin[i] + oth[i];
                const f32x2 ya = fma2(f32x2{hi2f(uu[i].y), lo2f(uu[i].y)}, f32x2{dval, dval}, y.xy);
                const f32x2 yb = fma2(f32x2{hi2f(uu[i].x), lo2f(uu[i].x)}, f32x2{dval, dval}, y.zw);
                const f32x2 xa = up2(x0[i].x), xb2 = up2(x0[i].y);
                const f32x2 za = fma2(fma2(xa, sa[i].xy, sb[i].xy), ya, xa), zb = fma2(fma2(xb2, sa[i].zw, sb[i].zw), yb, xb2);
                ST8S((v2u{pk2(za), pk2(zb)}), rws, off, OFF_X1 + i * 8 * 32768);
                ST8S((v2u{pk2(ya), pk2(yb)}), rws, off, OFF_Y + i * 8 * 32768);
                SB();
            }
        }
        STAMP(9);
        const int fb1 = tid < 8 ? tid * 1024 + b * 4 : OOB;
        VMW(0);
        BAR();
        FLAG(fb1, OFF_FL + FL_B);
        STAMP(10);
        __builtin_amdgcn_s_sleep(1);
        glds8(ws + w1o, lane * 16, (u32)(size_t)smem + L_W + w * 8192);
#pragma unroll
        for (int n = NW_CONV; n < NW_B; ++n) W1_LOAD(n);
        const int w2o = OFF_W2T + (cg * NP + w) * 4096, w2v = lane * 16 + 2048 * (w >> 1);
#define W2_LOAD(s, j) W2[s][j] = LD16S(rws, (s) == 10 && w == 3 ? OOB : (w2v ^ (((j) >> 1) * 2048)) + ((j) & 1) * 1024, w2o + (s) * 16384)
#pragma unroll
        for (int n = 0; n < NW2_PRE; ++n) W2_LOAD(n / 4, n % 4);
        const v4u fb = LD16S(rws, lane * 16, OFF_FL + FL_B + xc * 1024);
        SB();
        {
            v4u ones = {0x3f803f80u, 0x3f803f80u, 0x3f803f80u, 0x3f803f80u};
            PIN_V(ones);
            asm volatile("s_nop 1");
            f32x4 cs[3];
#pragma unroll
            for (int n = 0; n < NW_B; ++n) {
                if (n == NW_CONV) {
                    VMW(NW_B - NW_CONV + NW2_PRE + 1);
                    v4u wl[8];
#pragma unroll
                    for (int m = 0; m < 8; ++m) wl[m] = *(const LAS v4u*)(smem + L_W + w * 8192 + m * 1024 + lane * 16);
                    SB();
#pragma unroll
                    for (int m = 0; m < 8; ++m) MF(NW_CONV < 3 && m < 3 && (m + 1) % 3 >= NW_CONV, cs[m % 3], "v", wl[m], "v", ones);
                }
                if (n < NA1) MF(n < 3 && n < NW_CONV, cs[(n + 8) % 3], "a", W1[n], "v", ones);
                else MF(n < 3 && n < NW_CONV, cs[(n + 8) % 3], "v", W1[n], "v", ones);
                SB();
            }
            MF_DONE();
#pragma unroll
            for (int j = 0; j < 3; ++j) {
                PIN_V(cs[j]);
                *(LAS f32x4*)(smem + L_CS + (w * 3 + j) * 64 + g * 16) = cs[j];
            }
        }
        STAMP(11);
        POLL_ALL(fb, OFF_FL + FL_B + xc * 1024);
        STAMP(12);
        const u32 ldsb = (u32)(size_t)smem + w * 8192;
        glds8(ws + OFF_X1 + grp * 65536 + w * 8192, lane * 16, ldsb + L_X);
        FLAG(b == 0 && tid == 0 ? 0 : OOB, OFF_FL + FL_EPOCH);
        VMW(1);
    }
    f32x4 pst[4];
    v4u H[HR][2];
    auto ffn = [&](auto layer_) __attribute__((always_inline)) {
        constexpr int layer = decltype(layer_)::value;
        int bl = blockIdx.x, w = __builtin_amdgcn_readfirstlane(tid >> 6);
        asm volatile("" : "+s"(bl), "+s"(w));
        const int grp = 4 * (bl & 7) + (bl >> 6), cg = (bl >> 3) & 7;
        const int T0 = 11 * cg + 3 * w, Tend = cg == 7 ? NT : 11 * cg + 11;
        const u32 ldsb = (u32)(size_t)smem + w * 8192 + 4096;
        const char* gb = ws + grp * 65536 + w * 8192 + 4096;
        const int hoff = OFF_H + 2 * grp * NP * 2048;
        int lane = tid & 63;
        PIN_V(lane);
        const int c = lane & 15, g = lane >> 4;
        const int w2o = OFF_W2T + (cg * NP + w) * 4096, w2v = lane * 16 + 2048 * (w >> 1);
        const int w1o = OFF_W1T + (cg * 4 + w) * 49152;
        const int hv0 = (w * 2 + (w & 1)) * 1024 + lane * 16, hv1 = hv0 ^ 1024;
#define H_LOAD1(zz, i) H[(zz) % HR][i] = LD16S(rws, (zz) % 11 == 10 && w == 3 ? OOB : ((i) ? hv1 : hv0), hoff + (zz) / 11 * NP * 2048 + (zz) % 11 * 8192);
#define H_LOAD(zz)                                                                           \
    {                                                                                       \
        if ((zz) < 11 && 4 * (zz) + 4 > NW2_PRE + NW2_A + NW2_B && layer == 0) {              \
            _Pragma("unroll") for (int j = 0; j < 4; ++j)                                   \
                if (4 * (zz) + j >= NW2_PRE + NW2_A + NW2_B) W2_LOAD((zz) < 11 ? (zz) : 0, j); \
        }                                                                                   \
        H_LOAD1(zz, 0) H_LOAD1(zz, 1)                                                         \
    }
#define H_B3(p, q, r) __builtin_amdgcn_bitop3_b32(p, q, r, layer ? 0x80 : 0xfe)
#define H_BF(zz, p, q) __builtin_amdgcn_bitop3_b32(p, q, layer && (zz) % 11 == 10 && w == 3 ? 0u : 0x80008000u, layer ? 0x2a : 0xa8)
#define H_BAD(zz)                                                                              \
    H_BF(zz, H_B3(H[(zz) % HR][0].w, H[(zz) % HR][1].x, H[(zz) % HR][1].y),                    \
         H_B3(H[(zz) % HR][1].z, H[(zz) % HR][1].w, H_B3(H[(zz) % HR][0].x, H[(zz) % HR][0].y, H[(zz) % HR][0].z)))
#define H_RETRY(zz)                                                         \
    {                                                                       \
        u32 spins_ = 0;                                                     \
        while (!dead) {                                                     \
            __builtin_amdgcn_s_sleep(2);                                    \
            asm volatile("" ::: "memory");                                  \
            H_LOAD1(zz, 0) H_LOAD1(zz, 1)                                   \
            if (!__any(H_BAD(zz) != 0u)) break;                             \
            if (++spins_ > SPIN_CAP) dead = true;                           \
        }                                                                   \
    }
#define H_CHECK(zz) if (__builtin_expect(__any(H_BAD(zz) != 0u), 0)) H_RETRY(zz)
#define X2_PIECE(hf, n)                                                                                              \
    {                                                                                                                \
        if ((n) < 4) pst[(n) & 3] = __builtin_bit_cast(f32x4, LD16S(rws, lane * 16 + (n) * 1024, OFF_ST2 + (2 * grp + (hf)) * 4096)); \
        else glds1x((n) - 4, gb + OFF_X2 + (hf) * 32768, lane * 16, ldsb + L_X + (hf) * 32768);                      \
    }
#pragma unroll
        for (int hf = 0; hf < 2; ++hf) {
            STAMP(16 + layer * 24 + hf * 5);
            const LAS char* xb0[2];
#pragma unroll
            for (int i = 0; i < 2; ++i) xb0[i] = smem + L_X + hf * 32768 + (4 * g + (c >> 2)) * 64 + (((i ^ w ^ g) & 1) << 5) + 8 * (c & 3);
            v4u X[3][2], WL[8];
            f32x4 acc[3][2], accS, accG;
            v4u ones = {0x3f803f80u, 0x3f803f80u, 0x3f803f80u, 0x3f803f80u};
            PIN_V(ones);
#define G1_LOAD(kk, i)                                                                                          \
    {                                                                                                           \
        const LAS char* p_ = xb0[i] + ((((kk) >> 2) + w) & 3) * 8192 + ((kk) & 3) * 2048;                       \
        const v2u lo = tr_read(p_), hi = tr_read(p_ + 1024);                                                    \
        X[(kk) % 3][i] = v4u{lo.x, lo.y, hi.x, hi.y};                                                           \
    }
#define WL_LOAD(n) WL[n] = *(const LAS v4u*)(smem + L_W + w * 8192 + (n) * 1024 + lane * 16);
            if (layer == 1) {
                v4u ys[3], xs[8];
                LAS char* xp = smem + L_X + hf * 32768 + w * 8192 + lane * 16;
                if (hf == 0) VMW(3); else VMW(6);
                STAMP(17 + layer * 24 + hf * 5);
                f32x4 s4;
#define X2_READ()                                                                                   \
    _Pragma("unroll") for (int i = 0; i < 8; ++i) xs[i] = *(LAS v4u*)(xp + i * 1024);               \
    SB();                                                                                           \
    s4 = (pst[0] + pst[1]) + (pst[2] + pst[3]);                                                     \
    _Pragma("unroll") for (int e = 0; e < 4; ++e) s4[e] = xadd32(xadd16(s4[e]));
#define X2_BAD(m_)                                                                                  \
    u32 m_ = 0;                                                                                     \
    _Pragma("unroll") for (int i = 0; i < 8; ++i) m_ = max(max(max(m_, xs[i].x), xs[i].y), max(xs[i].z, xs[i].w)); \
    if (!((s4.x + s4.y) + (s4.z + s4.w) == (s4.x + s4.y) + (s4.z + s4.w))) m_ = ~0u;
                X2_READ()
#pragma unroll
                for (int i = 0; i < 3; ++i) ys[i] = *(LAS v4u*)(smem + L_Y + w * 8192 + lane * 16 + i * 1024);
                X2_BAD(mk)
                if (__builtin_expect(__any(mk == ~0u), 0)) {
                    u32 spins = 0;
                    while (!dead) {
                        __builtin_amdgcn_s_sleep(2);
#pragma unroll
                        for (int n = 0; n < 12; ++n) X2_PIECE(hf, n)
                        VMW(0);
                        X2_READ()
                        X2_BAD(mk2)
                        if (!__any(mk2 == ~0u)) break;
                        if (++spins > (SPIN_CAP >> 2)) dead = true;
                    }
                }
                SB();
                const f32x2 mean = f32x2{s4.x, s4.z} * (1.f / 512.f);
                const f32x2 var = f32x2{s4.y, s4.w} * (1.f / 512.f) - mean * mean;
                const f32x2 rs = {__builtin_amdgcn_rsqf(fmaxf(var.x, 0.f) + 1e-5f), __builtin_amdgcn_rsqf(fmaxf(var.y, 0.f) + 1e-5f)};
                LAS float* st = (LAS float*)(smem + L_MS + w * 256);
                *(LAS f32x2*)(st + 2 * (lane & 31)) = lane & 16 ? -mean * rs : rs;
                const int tok0 = 16 * (((lane >> 1) ^ g) & 1) + 8 * (lane & 1);
                const f32x4 a0 = *(LAS f32x4*)(st + tok0), a1 = *(LAS f32x4*)(st + tok0 + 4);
                const f32x4 b0 = *(LAS f32x4*)(st + 32 + tok0), b1 = *(LAS f32x4*)(st + 36 + tok0);
                f32x2 yf[2][4];
#pragma unroll
                for (int j = 0; j < 4; ++j) yf[0][j] = up2(ys[0][j]);
                SB();
#define G1_EARLY(n)                                                                                               \
    {                                                                                                             \
        const int kk_ = (n) >> 3, m_ = (n) & 7;                                                                   \
        if (m_ < 3) MF(kk_ == 0, acc[m_ % 3][0], "v", WL[kk_ * 3 + m_ % 3], "v", X[kk_][0]);                      \
        else if (m_ == 3) MF(kk_ == 0, accS, "v", ones, "v", X[kk_][0]);                                          \
        else if (m_ == 4) MF(kk_ == 0, accG, "v", X[kk_][0], "v", X[kk_][0]);                                     \
        else MF(kk_ == 0, acc[(m_ + 1) % 3][1], "v", WL[kk_ * 3 + (m_ + 1) % 3], "v", X[kk_][1]);                 \
    }
#pragma unroll
                for (int i = 0; i < 8; ++i) {
                    const bool mm = i >= 3 && i < 7;
                    f32x2 x[4], n[4];
#pragma unroll
                    for (int j = 0; j < 4; ++j) x[j] = up2(xs[i][j]);
                    asm volatile("" : "+v"(x[0]), "+v"(x[1]), "+v"(x[2]), "+v"(x[3]));
                    if (mm) G1_EARLY(4 * (i - 3))
                    if (i + 3 < 8) ys[i % 3] = *(LAS v4u*)(smem + L_Y + w * 8192 + lane * 16 + (i + 3) * 1024);
                    n[0] = fma2(x[0], a0.xy, b0.xy); n[1] = fma2(x[1], a0.zw, b0.zw);
                    n[2] = fma2(x[2], a1.xy, b1.xy); n[3] = fma2(x[3], a1.zw, b1.zw);
                    asm volatile("" : "+v"(n[0]), "+v"(n[1]), "+v"(n[2]), "+v"(n[3]));
                    if (mm) G1_EARLY(4 * (i - 3) + 1)
#pragma unroll
                    for (int j = 0; j < 4; ++j) n[j] = fma2(n[j], yf[i & 1][j], x[j]);
                    asm volatile("" : "+v"(n[0]), "+v"(n[1]), "+v"(n[2]), "+v"(n[3]));
                    if (mm) G1_EARLY(4 * (i - 3) + 2)
                    if (i < 7) {
#pragma unroll
                        for (int j = 0; j < 2; ++j) yf[(i + 1) & 1][j] = up2(ys[(i + 1) % 3][j]);
                        asm volatile("" : "+v"(yf[(i + 1) & 1][0]), "+v"(yf[(i + 1) & 1][1]));
                    }
                    v4u o;
#pragma unroll
                    for (int j = 0; j < 4; ++j) o[j] = pk2(n[j]);
                    PIN_V(o);
                    if (mm) G1_EARLY(4 * (i - 3) + 3)
                    if (i < 7) {
#pragma unroll
                        for (int j = 2; j < 4; ++j) yf[(i + 1) & 1][j] = up2(ys[(i + 1) % 3][j]);
                        asm volatile("" : "+v"(yf[(i + 1) & 1][2]), "+v"(yf[(i + 1) & 1][3]));
                    }
                    SB();
                    *(LAS v4u*)(xp + i * 1024) = o;
                    if (i & 1) { G1_LOAD(i >> 1, 0) G1_LOAD(i >> 1, 1) }
                    if (i == 1) { WL_LOAD(0) WL_LOAD(1) WL_LOAD(2) }
                    if (i == 3) { WL_LOAD(3) WL_LOAD(4) WL_LOAD(5) }
                    if (i == 6) { WL_LOAD(6) WL_LOAD(7) }
                    SB();
                }
            } else {
                if (hf == 1) VMW(6);
                G1_LOAD(0, 0) WL_LOAD(0) WL_LOAD(1) WL_LOAD(2) G1_LOAD(0, 1) G1_LOAD(1, 0) WL_LOAD(3) WL_LOAD(4) WL_LOAD(5) G1_LOAD(1, 1) WL_LOAD(6) WL_LOAD(7)
                SB();
            }
            STAMP(18 + layer * 24 + hf * 5);
            LAS f32x2* ls = (LAS f32x2*)(smem + L_ST + hf * 512);
            f32x2 s2[2][2];
            f32x4 ct[3];
            float rs[2], nb[2];
            const char* g8 = gb + (hf ? OFF_Y : layer ? OFF_Y + 32768 : OFF_X1 + 32768);
            const u32 l8 = ldsb + (hf || layer ? L_Y : L_X + 32768);
#define G1_MF(kk, j, i)                                                                                          \
    {                                                                                                            \
        const int n_ = (kk) * 3 + (j) - 8;                                                                       \
        if (n_ < 0) MF((kk) == 0, acc[j][i], "v", WL[n_ < 0 ? n_ + 8 : 0], "v", X[(kk) % 3][i]);                 \
        else if (n_ < NA1) MF_(acc[j][i], "a", W1[n_ < 0 ? 0 : n_], "v", X[(kk) % 3][i]);                        \
        else MF_(acc[j][i], "v", W1[n_ < 0 ? 0 : n_], "v", X[(kk) % 3][i]);                                      \
    }
#pragma unroll
            for (int kk = layer ? 2 : 0; kk < 16; ++kk) {
                if (kk == 2) BAR();
                G1_MF(kk, 0, 0)
                if (kk < 14) G1_LOAD(kk + 2, 0)
                SB();
                G1_MF(kk, 1, 0)
                if (kk < 14) G1_LOAD(kk + 2, 1)
                SB();
                G1_MF(kk, 2, 0)
                if (layer == 0 && kk < 8) glds1x(kk, g8, lane * 16, l8);
                if (layer == 1 && hf == 0 && kk < 8) X2_PIECE(1, 2 * (kk - 2))
                if (layer == 1 && hf == 0 && kk >= 8) glds1x(kk - 8, g8, lane * 16, l8);
                if (hf == 1 && kk >= 16 - HR) H_LOAD(kk - (16 - HR))
                SB();
                if (kk < 8) {
                    MF(kk == 0, accS, "v", ones, "v", X[kk % 3][0]);
                    MF(kk == 0, accG, "v", X[kk % 3][0], "v", X[kk % 3][0]);
                }
                if (kk == 9) {
                    PIN_V(accS); PIN_V(accG);
                    const float gd = (c & 2) ? ((c & 1) ? accG[3] : accG[2]) : ((c & 1) ? accG[1] : accG[0]);
                    ls[g == (c >> 2) ? w * 16 + c : 128 + lane] = f32x2{accS[0], gd};
                }
                if (kk == 11) {
#pragma unroll
                    for (int i = 0; i < 2; ++i) {
                        s2[i][0] = ls[((i ^ w) & 1) * 16 + c];
                        s2[i][1] = ls[(((i ^ w) & 1) + 2) * 16 + c];
                    }
#pragma unroll
                    for (int j = 0; j < 3; ++j) ct[j] = *(const LAS f32x4*)(smem + L_CS + (w * 3 + j) * 64 + g * 16);
                }
                if (kk == 13 || kk == 14) {
                    const int i = kk - 13;
                    const f32x2 s = (s2[i][0] + s2[i][1]) * (1.f / 512.f);
                    rs[i] = __builtin_amdgcn_rsqf(fmaxf(s.y - s.x * s.x, 0.f) + 1e-5f);
                    if (layer == 1) rs[i] = -rs[i];
                    nb[i] = -s.x * rs[i];
                    PIN_V(rs[i]); PIN_V(nb[i]);
                }
                SB();
                G1_MF(kk, 0, 1)
                if (layer == 1 && hf == 0 && kk < 8) X2_PIECE(1, 2 * (kk - 2) + 1)
                if (layer == 0) {
                    const int NL = hf ? 0 : 40 - NW_B, N0 = NW2_PRE + (hf ? NW2_A : 0), NN = NL + (hf ? NW2_B : NW2_A);
                    const int PER = (NN + WSTEPS - 1) / WSTEPS, n0 = kk * PER;
#pragma unroll
                    for (int e = 0; e < 6; ++e) {
                        const int n = n0 + e;
                        if (e < PER && n < NL) {
                            const int m = NW_B + n < 40 ? NW_B + n : 0;
                            W1[m] = LD16S(rws, T0 + (m + 8) % 3 < Tend ? lane * 16 : OOB, w1o + (m + 8) * 1024);
                        } else if (e < PER && n < NN) {
                            const int m = n >= NL && n < NN ? N0 + n - NL : 0;
                            W2_LOAD(m / 4, m % 4);
                        }
                    }
                }
                SB();
                G1_MF(kk, 1, 1)
                G1_MF(kk, 2, 1)
                if (kk == 10) BAR();
                SB();
            }
            STAMP(19 + layer * 24 + hf * 5);
            if (hf == 0 && NW_B < 40 && layer == 0) {
#pragma unroll
                for (int n = NW_B; n < 40; ++n) {
                    if (n < NA1) MF_(ct[(n + 8) % 3], "a", W1[n], "v", ones);
                    else MF_(ct[(n + 8) % 3], "v", W1[n], "v", ones);
                }
                MF_DONE();
#pragma unroll
                for (int j = 0; j < 3; ++j) {
                    PIN_V(ct[j]);
                    *(LAS f32x4*)(smem + L_CS + (w * 3 + j) * 64 + g * 16) = ct[j];
                }
            }
#pragma unroll
            for (int i = 0; i < 2; ++i) {
                f32x4 u[3];
#pragma unroll
                for (int j = 0; j < 3; ++j) u[j] = ct[j] * nb[i];
                asm volatile("" : "+v"(u[0]), "+v"(u[1]), "+v"(u[2]));
                if (i == 0) MF_DONE();
                asm volatile("" : "+v"(acc[0][i]), "+v"(acc[1][i]), "+v"(acc[2][i]));
#pragma unroll
                for (int j = 0; j < 3; ++j) u[j] = acc[j][i] * rs[i] + u[j];
                asm volatile("" : "+v"(u[0]), "+v"(u[1]), "+v"(u[2]));
                v2u o[3];
#pragma unroll
                for (int j = 0; j < 3; ++j) o[j] = v2u{pk2(u[j].xy), pk2(u[j].zw)};
                asm volatile("" : "+v"(o[0]), "+v"(o[1]), "+v"(o[2]));
#pragma unroll
                for (int j = 0; j < 3; ++j) {
                    const int T = T0 + j;
                    ST8S((v2u{relu2<layer>(o[j].x), relu2<layer>(o[j].y)}), rws,
                         T < Tend ? (((i ^ w) & 1) * 1024 + (g >> 1) * 256 + c * 16 + (g & 1) * 8) : OOB,
                         hoff + hf * NP * 2048 + (T >> 1) * 2048 + 512 * (T & 1));
                }
                SB();
            }
            STAMP(20 + layer * 24 + hf * 5);
        }
        H_CHECK(0)
        SB();
        STAMP(26 + layer * 24);
#pragma unroll
        for (int hf = 0; hf < 2; ++hf) {
            f32x4 acc[2][4];
            const int tt = w & 1, chl = 32 * (w >> 1) + 2 * c;
            const int xo = (64 * cg + chl) * 64 + ((tt ^ ((c >> 1) & 1)) << 5) + 8 * g;
            v2u res[2];
            LAS f32x4* red = (LAS f32x4*)(smem + L_RED);
#pragma unroll
            for (int s = 0; s < 11; ++s) {
                const int z = 11 * hf + s;
#define G2_MF(i, j) MF(s == 0, acc[i][j], "v", H[z % HR][i], "a", W2[s][j]);
                u32 b0 = 0, b1 = 0;
                G2_MF(1, 3)
                G2_MF(1, 2)
                if (s < 10) { b0 = H_B3(H[(z + 1) % HR][0].x, H[(z + 1) % HR][0].y, H[(z + 1) % HR][0].z); PIN_V(b0); }
                G2_MF(1, 1)
                if (s < 10) {
                    b1 = H_B3(H[(z + 1) % HR][0].w, H[(z + 1) % HR][1].x, H[(z + 1) % HR][1].y);
                    b0 = H_B3(H[(z + 1) % HR][1].z, H[(z + 1) % HR][1].w, b0);
                    asm volatile("" : "+v"(b0), "+v"(b1));
                }
                G2_MF(1, 0)
                bool bad = false;
                if (s < 10) bad = __any(H_BF(z + 1, b0, b1) != 0u);
                SB();
                G2_MF(0, 3)
                if (z + HR < 22) {
                    if (z + HR < 11 && 4 * (z + HR) + 4 > NW2_PRE + NW2_A + NW2_B && layer == 0) {
#pragma unroll
                        for (int j = 0; j < 4; ++j)
                            if (4 * (z + HR) + j >= NW2_PRE + NW2_A + NW2_B) W2_LOAD(z + HR < 11 ? z + HR : 0, j);
                    }
                    H_LOAD1(z + HR, 1)
                }
                SB();
                G2_MF(0, 2)
                SB();
                if (s < 10) { if (__builtin_expect(bad, 0)) H_RETRY(z + 1) }
                if (hf == 1 && layer == 0 && s >= 9) { X2_PIECE(0, 4 * (s - 9)) X2_PIECE(0, 4 * (s - 9) + 1) }
                SB();
                G2_MF(0, 1)
                G2_MF(0, 0)
                SB();
                if (hf == 1 && s == 4) BAR();
                if (z + HR < 22) H_LOAD1(z + HR, 0)
                if (hf == 1 && layer == 0 && s >= 9) { X2_PIECE(0, 4 * (s - 9) + 2) X2_PIECE(0, 4 * (s - 9) + 3) }
                if (s == 8) {
                    res[0] = *(const LAS v2u*)(smem + L_X + hf * 32768 + xo);
                    res[1] = *(const LAS v2u*)(smem + L_X + hf * 32768 + xo + 64);
                }
                SB();
            }
            STAMP(27 + layer * 24 + hf * 4);
#pragma unroll
            for (int n = 7; n >= 2; --n) {
                const int i = n >> 2, j = n & 3, src = i + 2 * (j >> 1);
                if (n == 7) asm volatile("s_nop 3" ::: "memory");
                if (n == 3) asm volatile("s_nop 3" ::: "memory");
                PIN_V(acc[i][j]);
                red[(((w ^ src) * 3 + src - 1) * 2 + (j & 1)) * 64 + lane] = acc[i][j];
                if (hf == 1 && layer == 0 && n >= 4) X2_PIECE(0, 15 - n)
                SB();
            }
            f32x2 xf[2][2];
            asm volatile("" : "+v"(acc[0][0]), "+v"(acc[0][1]));
#pragma unroll
            for (int j = 0; j < 2; ++j) {
                xf[j][0] = layer ? up2(res[j].x) - acc[0][j].xy : up2(res[j].x) + acc[0][j].xy;
                xf[j][1] = layer ? up2(res[j].y) - acc[0][j].zw : up2(res[j].y) + acc[0][j].zw;
            }
            BAR();
            f32x4 rd[6];
#pragma unroll
            for (int i = 0; i < 6; ++i) rd[i] = red[(w * 6 + i) * 64 + lane];
            SB();
            STAMP(28 + layer * 24 + hf * 4);
#pragma unroll
            for (int j = 0; j < 2; ++j) {
                const f32x4 t = rd[j] + (rd[2 + j] + rd[4 + j]);
                xf[j][0] = layer ? xf[j][0] - t.xy : xf[j][0] + t.xy;
                xf[j][1] = layer ? xf[j][1] - t.zw : xf[j][1] + t.zw;
            }
            STAMP(29 + layer * 24 + hf * 4);
            if (layer == 1) {
#pragma unroll
                for (int r = 0; r < 4; ++r)
                    __builtin_amdgcn_raw_buffer_store_b32(pk2bf(xf[0][r >> 1][r & 1], xf[1][r >> 1][r & 1]), mkrs(out, 2 * MB),
                                                          (16 * tt + 4 * g + r) * 1024 + chl * 2, (64 * grp + 32 * hf) * 1024 + cg * 128, 0);
            } else {
                u32 xq[2][2];
#pragma unroll
                for (int j = 0; j < 2; ++j) {
                    xq[j][0] = pk2(xf[j][0]);
                    xq[j][1] = pk2(xf[j][1]);
                    ST8S((v2u{xq[j][0], xq[j][1]}), rws, xo + 64 * j, OFF_X2 + (2 * grp + hf) * 32768);
                }
                f32x2 sm[2], sq[2];
#pragma unroll
                for (int r = 0; r < 2; ++r) {
                    const f32x2 x0 = up2(xq[0][r]), x1 = up2(xq[1][r]);
                    sm[r] = x0 + x1;
                    sq[r] = fma2(x0, x0, x1 * x1);
                }
                f32x4 s4;
                {
                    const bool odd = c & 1;
                    const f32x4 keep = odd ? f32x4{sm[1].x, sq[1].x, sm[1].y, sq[1].y} : f32x4{sm[0].x, sq[0].x, sm[0].y, sq[0].y};
                    const f32x4 give = odd ? f32x4{sm[0].x, sq[0].x, sm[0].y, sq[0].y} : f32x4{sm[1].x, sq[1].x, sm[1].y, sq[1].y};
#pragma unroll
                    for (int e = 0; e < 4; ++e) {
                        const float gv = give[e];
                        s4[e] = keep[e] + __builtin_bit_cast(float, __builtin_amdgcn_update_dpp(0, __builtin_bit_cast(int, gv), 0xb1, 0xf, 0xf, true));
                    }
#pragma unroll
                    for (int e = 0; e < 4; ++e) s4[e] = dppadd<0x122>(s4[e]);
#pragma unroll
                    for (int e = 0; e < 4; ++e) s4[e] = dppadd<0x124>(s4[e]);
#pragma unroll
                    for (int e = 0; e < 4; ++e) s4[e] = dppadd<0x128>(s4[e]);
                }
                ST16S(__builtin_bit_cast(v4u, s4), rws, c < 2 ? (cg * 2 + (w >> 1)) * 256 + (16 * tt + 4 * g + 2 * c) * 8 : OOB,
                      OFF_ST2 + (2 * grp + hf) * 4096);
            }
            SB();
            if (hf == 0) H_CHECK(11)
            STAMP(30 + layer * 24 + hf * 4);
        }
    };
    ffn(IC<0>{});
    ffn(IC<1>{});
    asm volatile(".Lpf_end:");
}

extern "C" void kernel_launch(void* const* d_in, const int* in_sizes, int n_in, void* d_out, int out_size, void* d_ws,
                              size_t ws_size, hipStream_t stream) {
    constexpr int lds = L_BYTES;
    (void)hipFuncSetAttribute((const void*)k_all, hipFuncAttributeMaxDynamicSharedMemorySize, lds);
    k_all<<<256, 256, lds, stream>>>((const u16*)d_in[0], (const u16*)d_in[1], (const u16*)d_in[2], (const u16*)d_in[3],
                                     (const u16*)d_in[4], (const u16*)d_in[5], (const u16*)d_in[7], (const u16*)d_in[8],
                                     (u16*)d_out, (char*)d_ws);
}
```

```cpp
#include <hip/hip_runtime.h>
#include <hip/hip_bf16.h>
#include <stdint.h>

typedef unsigned short u16;
typedef unsigned int u32;
typedef unsigned long long u64;
typedef u32 v4u __attribute__((ext_vector_type(4)));
typedef u32 v2u __attribute__((ext_vector_type(2)));
typedef float f32x4 __attribute__((ext_vector_type(4)));
typedef float f32x2 __attribute__((ext_vector_type(2)));
typedef short bf16x8 __attribute__((ext_vector_type(8)));
typedef short s16x4 __attribute__((ext_vector_type(4)));
typedef short s16x2 __attribute__((ext_vector_type(2)));
typedef __bf16 bf16x2 __attribute__((ext_vector_type(2)));
#define LAS __attribute__((address_space(3)))
#define GAS __attribute__((address_space(1)))
#define DEV __device__ __forceinline__
#define RLX __ATOMIC_RELAXED, __HIP_MEMORY_SCOPE_AGENT
typedef __amdgpu_buffer_rsrc_t rsrc_t;

constexpr int S_ = 2048, D_ = 512, F_ = 1376;
constexpr int NP = 43;
constexpr int NT = 86;
constexpr int MB = 1 << 20;
constexpr int OFF_FL = 0, OFF_ST0 = 512 * 1024;
constexpr int OFF_UR = 1 * MB, OFF_X0T = 3 * MB, OFF_Y = 5 * MB, OFF_X1 = 7 * MB, OFF_W1T = 11 * MB,
              OFF_W2T = 13 * MB, OFF_H = 15 * MB, OFF_X2 = OFF_H + 64 * NP * 2048, OFF_ST2 = OFF_X2 + 2 * MB, WS_USED = 27 * MB;
static_assert(OFF_ST2 + 64 * 4096 - OFF_H <= 256 * 31 * 1024, "the marked region is 31 KiB per workgroup");
constexpr int FL_EPOCH = 0, FL_A = 4096, FL_A2 = 40960, FL_B = 49152, FL_W = 57344, FL_G = 81920;
constexpr int SC1 = 16, OOB = 0x40000000;
constexpr int L_X = 0, L_Y = 65536, L_W = 98304, L_RED = 131072, L_ST = 155648, L_MS = L_ST + 2560, L_CS = L_MS + 1024, L_BYTES = L_CS + 1024;
constexpr int CV_E = 0, CV_O = 4160, CV_U = 8320, CV_CH = 12928, CV_X = 2 * CV_CH, L_P0 = 49152;
constexpr u32 SPIN_CAP = 1u << 18;
#define NA1 20
#define HR 6
#define PSLEEP 2
#define SECMODE 0
#define QAUX 2
#define NTAUX 2
#define WAUX 2
#define A2R 1
#define A1DELAY 12
#define WPOS 2
#define NPRE 3
#define CPOLL 12
#define CPOLL2 8
#define DPOLL 5
#define DCHK 10
#define MIDMIX 3
#define CODEPF 1
#define QEARLY 1
#define SECDELAY 1
#define PUBI -1
#define POLI 4
#define EARLYDELAY 0
#define WSTEPS 9
#define NPRE2 4
#define DPOLL2 10
#define ENDINV 0
#define CLRPOS 1
#define FKK 6
#define XKK 11
#define PKK 3
#define SQ 7
#define XSLEEP 6
#define PSLEEP2 4
#define STAG 1
#define PACE 0
#define BPOLL 10
#define Q_CONV 8
#define Q_Y 8
#define Q_B 34
#define Q_X 34
#define Q_A 49
#define Q_E 49
#define Q_H 76
#define WCD 2
static_assert(Q_Y >= 3 && Q_CONV <= Q_Y && Q_Y < Q_B && Q_B <= Q_X && Q_X <= Q_A && Q_A <= Q_E && Q_E <= Q_H && Q_H <= 84 && Q_A >= 40, "order");
static_assert(24 + Q_CONV * WCD <= (3 - A2R) * 36, "slots");
constexpr bool late_ok() {
    const int per = (Q_A - Q_X + WSTEPS - 1) / WSTEPS;
    for (int n = Q_X; n < 40; ++n)
        if (2 + (n - Q_X) / per >= (n + 8) / 3) return false;
    return per <= 6 && (Q_H - Q_E + WSTEPS - 1) / WSTEPS <= 6 && WSTEPS <= 14;
}
static_assert(!PACE || (Q_B - Q_Y >= PACE && BPOLL < 8 + Q_B - Q_Y), "pace");
static_assert(late_ok(), "a fragment would be used before it is requested");
constexpr int NCS = Q_B < 40 ? Q_B : 40;
template <int N> struct IC { static constexpr int value = N; };
DEV float lo2f(u32 d) { return __builtin_bit_cast(float, d << 16); }
DEV float hi2f(u32 d) { return __builtin_bit_cast(float, d & 0xffff0000u); }
DEV f32x2 up2(u32 d) { return f32x2{lo2f(d), hi2f(d)}; }
DEV u32 pk2(f32x2 x) { return __builtin_bit_cast(u32, __builtin_convertvector(x, bf16x2)); }
DEV u32 pk2bf(float a, float b) { return pk2(f32x2{a, b}); }
DEV f32x2 fma2(f32x2 a, f32x2 b, f32x2 c) { return __builtin_elementwise_fma(a, b, c); }
DEV f32x4 mfma(v4u a, v4u b, f32x4 c) {
    return __builtin_amdgcn_mfma_f32_16x16x32_bf16(__builtin_bit_cast(bf16x8, a), __builtin_bit_cast(bf16x8, b), c, 0, 0, 0);
}
DEV v2u tr_read(const LAS char* p) {
    return __builtin_bit_cast(v2u, __builtin_amdgcn_ds_read_tr16_b64_v4i16((LAS s16x4*)p));
}
DEV float xadd16(float v) {
    auto r = __builtin_amdgcn_permlane16_swap(__builtin_bit_cast(u32, v), __builtin_bit_cast(u32, v), false, false);
    const u32 r0 = r[0], r1 = r[1];
    return __builtin_bit_cast(float, r0) + __builtin_bit_cast(float, r1);
}
DEV float xadd32(float v) {
    auto r = __builtin_amdgcn_permlane32_swap(__builtin_bit_cast(u32, v), __builtin_bit_cast(u32, v), false, false);
    const u32 r0 = r[0], r1 = r[1];
    return __builtin_bit_cast(float, r0) + __builtin_bit_cast(float, r1);
}
template <int CTRL> DEV float dppadd(float v) {
    return v + __builtin_bit_cast(float, __builtin_amdgcn_update_dpp(0, __builtin_bit_cast(int, v), CTRL, 0xf, 0xf, true));
}
DEV float row16sum(float v) {
    v = dppadd<0x121>(v);
    v = dppadd<0x122>(v);
    v = dppadd<0x124>(v);
    v = dppadd<0x128>(v);
    return v;
}
typedef unsigned short u16x2 __attribute__((ext_vector_type(2)));
template <int NEG> DEV u32 relu2(u32 v) {
    if (NEG) return __builtin_bit_cast(u32, __builtin_elementwise_max(__builtin_bit_cast(u16x2, v), u16x2{0x8000, 0x8000}));
    return __builtin_bit_cast(u32, __builtin_elementwise_max(__builtin_bit_cast(s16x2, v), s16x2{0, 0}));
}
DEV rsrc_t mkrs(const void* p, int bytes) { return __builtin_amdgcn_make_buffer_rsrc((void*)p, 0, bytes, 0x00020000); }
#define LD16(r, vo, so) __builtin_amdgcn_raw_buffer_load_b128(r, vo, so, QAUX)
#define LD16N(r, vo, so) __builtin_amdgcn_raw_buffer_load_b128(r, vo, so, NTAUX)
#define LD4(r, vo, so) __builtin_amdgcn_raw_buffer_load_b32(r, vo, so, WAUX)
#define LD16S(r, vo, so) __builtin_amdgcn_raw_buffer_load_b128(r, vo, so, SC1)
#define LD8S(r, vo, so) __builtin_amdgcn_raw_buffer_load_b64(r, vo, so, SC1)
#define ST16S(v, r, vo, so) __builtin_amdgcn_raw_buffer_store_b128(v, r, (vo) + (so), 0, SC1)
#define ST8S(v, r, vo, so) __builtin_amdgcn_raw_buffer_store_b64(v, r, vo, so, SC1)
#define SB() __builtin_amdgcn_sched_barrier(0)
#define MF_(acc, ca, a, cb, b) asm volatile("v_mfma_f32_16x16x32_bf16 %0, %1, %2, %0" : "+v"(acc) : ca(a), cb(b))
#define MF0_(acc, ca, a, cb, b) asm volatile("v_mfma_f32_16x16x32_bf16 %0, %1, %2, 0" : "=&v"(acc) : ca(a), cb(b))
#define MF(first, acc, ca, a, cb, b) do { if (first) MF0_(acc, ca, a, cb, b); else MF_(acc, ca, a, cb, b); } while (0)
#define MF_DONE() asm volatile("s_nop 7" ::: "memory")
#define PIN_A(x) asm volatile("" : "+a"(x))
#define PIN_V(x) asm volatile("" : "+v"(x))
#define BAR() do { SB(); asm volatile("s_waitcnt lgkmcnt(0)\n\ts_barrier" ::: "memory"); SB(); } while (0)
#define VMW(n) do { SB(); asm volatile("s_waitcnt vmcnt(%0)" ::"n"(n) : "memory"); SB(); } while (0)
DEV void glds8(const char* sbase, u32 voff, u32 lds) {
    u32 keep;
    asm volatile(
        "s_mov_b32 %0, m0\n\ts_mov_b32 m0, %3\n\ts_nop 0\n\t"
        "global_load_lds_dwordx4 %1, %2 offset:-4096 sc1\n\t"
        "global_load_lds_dwordx4 %1, %2 offset:-3072 sc1\n\t"
        "global_load_lds_dwordx4 %1, %2 offset:-2048 sc1\n\t"
        "global_load_lds_dwordx4 %1, %2 offset:-1024 sc1\n\t"
        "global_load_lds_dwordx4 %1, %2 sc1\n\t"
        "global_load_lds_dwordx4 %1, %2 offset:1024 sc1\n\t"
        "global_load_lds_dwordx4 %1, %2 offset:2048 sc1\n\t"
        "global_load_lds_dwordx4 %1, %2 offset:3072 sc1\n\t"
        "s_mov_b32 m0, %0"
        : "=&s"(keep) : "v"(voff), "s"(sbase + 4096), "s"(lds + 4096) : "memory");
}
DEV void glds4(const char* sbase, u32 voff, u32 lds) {
    u32 keep;
    asm volatile(
        "s_mov_b32 %0, m0\n\ts_mov_b32 m0, %3\n\ts_nop 0\n\t"
        "global_load_lds_dwordx4 %1, %2 sc1\n\t"
        "global_load_lds_dwordx4 %1, %2 offset:1024 sc1\n\t"
        "global_load_lds_dwordx4 %1, %2 offset:2048 sc1\n\t"
        "global_load_lds_dwordx4 %1, %2 offset:3072 sc1\n\t"
        "s_mov_b32 m0, %0"
        : "=&s"(keep) : "v"(voff), "s"(sbase), "s"(lds) : "memory");
}
template <int OFF> DEV void glds1(const char* sbase, u32 voff, u32 lds) {
    u32 keep;
    asm volatile("s_mov_b32 %0, m0\n\ts_mov_b32 m0, %3\n\ts_nop 0\n\tglobal_load_lds_dwordx4 %1, %2 offset:%4 sc1\n\ts_mov_b32 m0, %0"
                 : "=&s"(keep) : "v"(voff), "s"(sbase), "s"(lds), "n"(OFF) : "memory");
}
DEV void glds1x(int i, const char* sbase, u32 voff, u32 lds) {
    switch (i) {
        case 0: glds1<-4096>(sbase, voff, lds); break;
        case 1: glds1<-3072>(sbase, voff, lds); break;
        case 2: glds1<-2048>(sbase, voff, lds); break;
        case 3: glds1<-1024>(sbase, voff, lds); break;
        case 4: glds1<0>(sbase, voff, lds); break;
        case 5: glds1<1024>(sbase, voff, lds); break;
        case 6: glds1<2048>(sbase, voff, lds); break;
        default: glds1<3072>(sbase, voff, lds); break;
    }
}
DEV u32 mktag(u32 e) { return (u32)__builtin_amdgcn_readfirstlane(e) * 1664525u + 1013904223u; }
#define LD4S(vo, so) __builtin_amdgcn_raw_buffer_load_b32(rws, vo, so, SC1)
#define FLAG(vo, so) __builtin_amdgcn_raw_buffer_store_b32(tag, rws, vo, so, SC1)
#define POLL_FINISH(f, vo, so)                                            \
    do {                                                                  \
        if (!__all(f == tag)) {                                           \
            u32 spins_ = 0;                                               \
            bool ok_ = f == tag;                                          \
            while (!dead) {                                               \
                __builtin_amdgcn_s_sleep(PSLEEP);                         \
                asm volatile("" ::: "memory");                            \
                const u32 g_ = LD4S(ok_ ? OOB : (vo), so);                \
                ok_ = ok_ || g_ == tag;                                   \
                if (__all(ok_)) break;                                    \
                if (++spins_ > SPIN_CAP) dead = true;                     \
            }                                                             \
        }                                                                 \
        __builtin_amdgcn_fence(__ATOMIC_ACQUIRE, "wavefront");            \
        SB();                                                             \
    } while (0)
#define ALLOK(P) __all(P.x == tag && P.y == tag && P.z == tag && P.w == tag)
#define POLL_ALL(f, so)                                                   \
    do {                                                                  \
        if (!ALLOK(f)) {                                                  \
            u32 spins_ = 0;                                               \
            while (!dead) {                                               \
                __builtin_amdgcn_s_sleep(8);                              \
                asm volatile("" ::: "memory");                            \
                const v4u g_ = LD16S(rws, lane * 16, so);                 \
                if (ALLOK(g_)) break;                                     \
                if (++spins_ > SPIN_CAP) dead = true;                     \
            }                                                             \
        }                                                                 \
        __builtin_amdgcn_fence(__ATOMIC_ACQUIRE, "wavefront");            \
        SB();                                                             \
    } while (0)
#define STAMP(i)

__global__ __launch_bounds__(256, 1) void k_all(const u16* __restrict__ q, const u16* __restrict__ k, const u16* __restrict__ v,
                                                const u16* __restrict__ sk, const u16* __restrict__ ssmk,
                                                const u16* __restrict__ Dv, const u16* __restrict__ w1,
                                                const u16* __restrict__ w2, u16* __restrict__ out, char* __restrict__ ws) {
    extern __shared__ __attribute__((aligned(16))) char smem_[];
    LAS char* smem = (LAS char*)smem_;
    const int b = blockIdx.x, tid = threadIdx.x, w = __builtin_amdgcn_readfirstlane(tid >> 6);
    const int xc = b & 7, jb = b >> 3;
    const rsrc_t rws = mkrs(ws, WS_USED);
    bool dead = false;
    STAMP(0);
    const int ch0 = 64 * xc + 2 * jb;
    const int grp = 4 * xc + (b >> 6), cg = jb & 7;
    const int T0 = 11 * cg + 3 * w, Tend = cg == 7 ? NT : 11 * cg + 11;
    u32 tag;
    v4u W1[40], W2[11][4];
    {
        const int lane = tid & 63, c = lane & 15, g = lane >> 4;
#define Q_LOADS()                                                                                              \
    _Pragma("unroll") for (int i = 0; i < 4; ++i) hq[i] = LD16(r2, (w < 2 ? lane * 16 : qvo) + i * 1024, 0);   \
    _Pragma("unroll") for (int i = 0; i < 4; ++i) xr[i] = LD16(r2, (8 * b + 4 * half + 4 + i) * 1024 + lane * 16 - (w < 2 ? 0 : 4096), 0);
#define W_LOADS()                                                                                              \
    _Pragma("unroll") for (int i = 0; i < 8; ++i)                                                              \
        wr[i] = __builtin_amdgcn_raw_buffer_load_b128(                                                         \
            rw, wvo, is1 ? (16 * (i & 1) + 4 * (i >> 1)) * F_ * 2 : (16 * ((i >> 1) & 1) + (i >> 2) + 8 * (i & 1)) * D_ * 2, WAUX);
        u32 pf;
        {
            u64 pc;
            u32 len;
            asm volatile("s_getpc_b64 %0\n.Lpf_base:\n\ts_mov_b32 %1, .Lpf_end-.Lpf_base" : "=s"(pc), "=s"(len));
            pf = __builtin_amdgcn_raw_buffer_load_b32(mkrs((const void*)pc, len), w == 3 && lane < 14 ? jb * 1792 + lane * 128 : OOB, 0, 0);
        }
        const int cw = w >> 1, half = w & 1, ch = ch0 + cw;
        u32 draw;
        {
            const int row = 2 * (tid >> 3), s0 = 64 * jb + row;
            const int vo = s0 * 1024 + xc * 128 + (lane & 7) * 16;
            const rsrc_t rk = mkrs(k, 2 * MB), rv = mkrs(v, 2 * MB);
            v4u kk[5], vv[2], sw[4];
#pragma unroll
            for (int t = 0; t < 5; ++t) kk[t] = LD16N(rk, s0 + t - 3 >= 0 ? vo + (t - 3) * 1024 : OOB, 0);
#pragma unroll
            for (int t = 0; t < 2; ++t) vv[t] = LD16N(rv, vo + t * 1024, 0);
#pragma unroll
            for (int t = 0; t < 4; ++t) sw[t] = *(const v4u*)(sk + (64 * xc + 8 * (lane & 7)) * 4 + 8 * t);
            const u32 epoch = LD4S(0, OFF_FL + FL_EPOCH);
            draw = __builtin_amdgcn_raw_buffer_load_b16(mkrs(Dv, 1024), 0, ch * 2, 0);
            const rsrc_t r2 = mkrs(w < 2 ? ssmk + (ch0 + half) * S_ : q, w < 2 ? 4096 : 2 * MB);
            const int qvo = (64 * jb + 32 * half + 4 * (lane >> 3)) * 1024 + xc * 128 + (lane & 7) * 16;
            v4u hq[4], xr[4], wr[8];
            const int wu = 2 * b + half;
            const bool is1 = wu < 176;
            const int u11 = (wu * 5958) >> 16, ua = is1 ? u11 : (wu - 176) >> 2, ub = is1 ? wu - 11 * u11 : wu & 3;
            const int rowb = is1 ? F_ * 2 : D_ * 2;
            const rsrc_t rw = mkrs(is1 ? w1 : w2, wu < 348 && w >= 2 ? F_ * D_ * 2 : 0);
            const int wvo = (32 * ua + (g << (is1 ? 0 : 1))) * rowb + ub * 256 + c * 16;
            Q_LOADS()
            W_LOADS()
            int wdst;
            {
                const int T = 8 * ub + lane, tc = (T * 5958) >> 16, tr = T - 11 * tc, tw = (tr * 22) >> 6, tj = tr - 3 * tw;
                const int o1 = OFF_W1T + (tc * 4 + tw) * 49152 + (((ua - 4 * tw) & 15) * 3 + tj) * 1024;
                const int o2 = OFF_W2T + ((2 * ub + (lane >> 2)) * NP + ua) * 4096 + (lane & 3) * 1024;
                wdst = T < (is1 ? NT : wu < 348 ? 1024 : 0) ? (is1 ? o1 : o2) : OOB;
                PIN_V(wdst);
            }
            SB();
#define W_STORES()                                                                                          \
    {                                                                                                       \
        LAS char* wl = smem + L_Y + half * 16384;                                                           \
        _Pragma("unroll") for (int i = 0; i < 8; ++i) *(LAS v4u*)(wl + i * 1024 + (i >> 1) * 64 + lane * 16) = wr[i]; \
        _Pragma("unroll") for (int blk = 0; blk < 4; ++blk) {                                               \
            u32 d[8];                                                                                       \
            _Pragma("unroll") for (int e = 0; e < 8; ++e) d[e] = *(const LAS u32*)(wl + g * 2112 + c * 4 + e * 256 + blk * 64); \
            _Pragma("unroll") for (int u = 0; u < 2; ++u) {                                                 \
                v4u fr;                                                                                     \
                _Pragma("unroll") for (int e = 0; e < 4; ++e)                                               \
                    fr[e] = __builtin_amdgcn_perm(d[2 * e + 1], d[2 * e], u ? 0x07060302u : 0x05040100u);  \
                ST16S(fr, rws, lane * 16, __builtin_amdgcn_readlane(wdst, 2 * blk + u));                                            \
            }                                                                                               \
        }                                                                                                   \
    }
#pragma unroll
            for (int t = 0; t < 2; ++t) {
                v4u o;
#pragma unroll
                for (int j = 0; j < 4; ++j) {
                    f32x2 a = up2(kk[t][j]) * f32x2{hi2f(sw[j].y), hi2f(sw[j].w)};
                    a = fma2(up2(kk[t + 1][j]), f32x2{lo2f(sw[j].y), lo2f(sw[j].w)}, a);
                    a = fma2(up2(kk[t + 2][j]), f32x2{hi2f(sw[j].x), hi2f(sw[j].z)}, a);
                    a = fma2(up2(kk[t + 3][j]), f32x2{lo2f(sw[j].x), lo2f(sw[j].z)}, a);
                    o[j] = pk2(a * up2(vv[t][j]));
                }
                *(LAS v4u*)(smem + L_P0 + (row + t) * 128 + (lane & 7) * 16) = o;
            }
            tag = mktag(epoch);
            PIN_V(draw);
            PIN_V(pf);
            BAR();
            STAMP(1);
            const int qd = c >> 2;
            if (w < 2) {
                const LAS char* base = smem + L_P0 + 64 * half + 8 * (c & 3) + (63 - 8 * g - qd) * 128;
                const int dst = (64 * xc + 32 * half + c) * 4096 + 16 * g + OFF_UR + (1984 - 64 * jb) * 2;
                v2u a[8];
#pragma unroll
                for (int i = 0; i < 8; ++i) a[i] = tr_read(base + (i >> 2) * 32 - (32 * ((i >> 1) & 1) + 4 * (i & 1)) * 128);
#pragma unroll
                for (int i = 0; i < 4; ++i)
                    ST16S((v4u{a[2 * i].x, a[2 * i].y, a[2 * i + 1].x, a[2 * i + 1].y}), rws, dst, (i & 1) * 64 + (i >> 1) * 65536);
                SB();
                LAS char* cv = smem + half * CV_CH;
                const v4u zero = {0, 0, 0, 0};
                if (lane < 33) *(LAS v4u*)(cv + (lane < 30 ? CV_U + 4096 + lane * 16 : lane < 32 ? CV_E + 16 * (lane - 30) : CV_O)) = zero;
                if (lane == 0) *(LAS v4u*)(cv + CV_O + 16) = v4u{0, 0, 0, hq[0].x << 16};
#pragma unroll
                for (int i = 0; i < 4; ++i) {
                    const u32 nx = i < 3 ? __builtin_amdgcn_readlane(hq[i < 3 ? i + 1 : i].x, 0) : 0u;
                    v4u o;
                    o.x = __builtin_amdgcn_alignbyte(hq[i].y, hq[i].x, 2);
                    o.y = __builtin_amdgcn_alignbyte(hq[i].z, hq[i].y, 2);
                    o.z = __builtin_amdgcn_alignbyte(hq[i].w, hq[i].z, 2);
                    o.w = __builtin_amdgcn_alignbyte((u32)__builtin_amdgcn_update_dpp((int)nx, (int)hq[i].x, 0x130, 0xf, 0xf, false), hq[i].w, 2);
                    *(LAS v4u*)(cv + CV_E + 32 + lane * 16 + i * 1024) = hq[i];
                    *(LAS v4u*)(cv + CV_O + 32 + lane * 16 + i * 1024) = o;
                }
                VMW(0);
#pragma unroll
                for (int i = 0; i < 4; ++i) PIN_V(xr[i]);
#pragma unroll
                for (int i = 0; i < 8; ++i) PIN_V(wr[i]);
                STAMP(2);
                volatile LAS u32* hs = (volatile LAS u32*)(smem + L_ST);
                hs[half] = tag;
                FLAG(hs[half ^ 1] == tag && lane == 0 ? (xc * 32 + jb) * 128 : OOB, OFF_FL + FL_A);
                if (w == 0) {
                    const int fo = OFF_FL + FL_A + xc * 4096, fv = lane < 32 ? lane * 128 : OOB;
                    __builtin_amdgcn_s_sleep(A1DELAY);
                    u32 f = LD4S(fv, fo);
                    if (lane >= 32) f = tag;
                    POLL_FINISH(f, fv, fo);
                    STAMP(5);
                    glds4(ws + OFF_UR + ch0 * 4096, lane * 16, (u32)(size_t)(smem + CV_U));
                    glds4(ws + OFF_UR + ch0 * 4096 + 4096, lane * 16, (u32)(size_t)(smem + CV_CH + CV_U));
                }
            } else {
                __builtin_amdgcn_s_sleep(SECDELAY);
                SB();
                LAS char* img = smem + L_P0 + 8192 + half * 4096;
#pragma unroll
                for (int t = 0; t < 4; ++t) *(LAS v4u*)(img + (4 * (lane >> 3) + t) * 128 + (lane & 7) * 16) = hq[t];
                v2u a[8];
#pragma unroll
                for (int i = 0; i < 8; ++i) a[i] = tr_read(img + 8 * (c & 3) + (i >> 1) * 32 + (4 * (i & 1) + 8 * g + qd) * 128);
#pragma unroll
                for (int i = 0; i < 4; ++i)
                    ST16S((v4u{a[2 * i].x, a[2 * i].y, a[2 * i + 1].x, a[2 * i + 1].y}), rws, (64 * xc + c) * 4096 + 16 * g,
                          OFF_X0T + 128 * jb + 64 * half + i * 65536);
                SB();
                f32x4 sta, stb;
#pragma unroll
                for (int i = 0; i < 4; ++i) {
                    f32x2 x[4];
#pragma unroll
                    for (int j = 0; j < 4; ++j) x[j] = up2(xr[i][j]);
                    const f32x2 s2v = (x[0] + x[1]) + (x[2] + x[3]);
                    const f32x2 q2v = fma2(x[0], x[0], x[1] * x[1]) + fma2(x[2], x[2], x[3] * x[3]);
                    sta[i] = s2v.x + s2v.y;
                    stb[i] = q2v.x + q2v.y;
                }
#pragma unroll
                for (int i = 0; i < 4; ++i) { sta[i] = row16sum(sta[i]); stb[i] = row16sum(stb[i]); }
#pragma unroll
                for (int i = 0; i < 4; ++i) {
                    const float mean = xadd32(xadd16(sta[i])) * (1.f / 512.f);
                    const float rs = __builtin_amdgcn_rsqf(fmaxf(xadd32(xadd16(stb[i])) * (1.f / 512.f) - mean * mean, 0.f) + 1e-5f);
                    sta[i] = rs;
                    stb[i] = -mean * rs;
                }
                ST16S(__builtin_bit_cast(v4u, lane ? stb : sta), rws, lane < 2 ? (8 * b + 4 * half) * 4 + lane * 8192 : OOB, OFF_ST0);
                SB();
                W_STORES()
                STAMP(3);
#define A2_PUBLISH()                                                                              \
    {                                                                                             \
        VMW(0);                                                                                   \
        volatile LAS u32* hs = (volatile LAS u32*)(smem + L_ST + 8);                              \
        hs[half] = tag;                                                                           \
        FLAG(hs[half ^ 1] == tag && lane < 8 ? lane * 1024 + b * 4 : OOB, OFF_FL + FL_A2);        \
    }
                A2_PUBLISH()
                STAMP(4);
            }
        }
        const LAS char* ab = smem + cw * CV_CH + ((c & 1) ? CV_E + 2 * (1 + c) : CV_O + 2 * c) + 16 * g + 256 * half;
        const LAS char* bb = smem + cw * CV_CH + CV_U + 4064 - 3584 - 32 * c + 16 * g + 256 * half;
        v4u A[2][8], B[2][8];
#define CV_LA(buf, r, i) { const LAS u32* pa = (const LAS u32*)(ab + 512 * (i) + 64 * (r)); A[buf][i] = v4u{pa[0], pa[1], pa[2], pa[3]}; }
#define CV_LB(buf, r, i) B[buf][i] = *(const LAS v4u*)(bb + 512 * (7 - (i)) + 64 * (r));
        if (w == 0) VMW(0);
        BAR();
        STAMP(6);
        v4u fa;
        CV_LA(0, 0, 0)
#pragma unroll
        for (int i = 0; i < 8; ++i) CV_LB(0, 0, i)
#pragma unroll
        for (int i = 1; i < 8; ++i) CV_LA(0, 0, i)
        f32x4 acc[8];
        v2u x0[4], uu[4];
        f32x4 sa[4], sb[4];
        const int w1o = OFF_W1T + (cg * 4 + w) * 49152;
        int w1v[3];
#pragma unroll
        for (int j = 0; j < 3; ++j) w1v[j] = T0 + j < Tend ? j * 1024 + lane * 16 : OOB;
        const int tk = 1024 * half + 16 * c + 4 * g;
        const int w2o = OFF_W2T + (cg * NP + w) * 4096, w2v = lane * 16 + 2048 * (w >> 1);
#define W2_LOAD(s, j) W2[s][j] = LD16S(rws, (s) == 10 && w == 3 ? OOB : (w2v ^ (((j) >> 1) * 2048)) + ((j) & 1) * 1024, w2o + (s) * 16384)
#define WSEQ(n)                                                                                                      \
    {                                                                                                                \
        if ((n) < 40) W1[(n) < 40 ? (n) : 0] = LD16S(rws, w1v[((n) + 8) % 3], w1o + (((n) + 8) / 3) * 3072);         \
        else W2_LOAD(((n) < 40 ? 0 : (n) - 40) / 4, ((n) < 40 ? 0 : (n) - 40) % 4);                                  \
    }
        auto conv_step = [&](auto r_) __attribute__((always_inline)) {
            constexpr int r = decltype(r_)::value;
            int idx = 0;
#pragma unroll
            for (int al = 0; al < 8; ++al)
#pragma unroll
                for (int be = 0; be + al < 8; ++be) {
                    MF(r == 0 && al == 0, acc[al + be], "v", A[r & 1][al], "v", B[r & 1][be]);
                    if (r < 3 && idx >= 4 && idx < 20) {
                        if (idx & 1) CV_LA((r + 1) & 1, r + 1, (idx - 4) >> 1) else CV_LB((r + 1) & 1, r + 1, (idx - 4) >> 1)
                    }
                    if (r == 3 && idx < 4) uu[idx] = *(const LAS v2u*)(smem + cw * CV_CH + CV_U + 2 * (2044 - tk) - 512 * idx);
                    if (r == CLRPOS - 1 && idx % 12 == 2)
                        ST16S((v4u{~0u, ~0u, ~0u, ~0u}), rws, lane * 16,
                              idx < 24 ? OFF_X2 + grp * 65536 + cg * 8192 + (idx / 12 * 4 + w) * 1024 : w == 0 ? OFF_ST2 + grp * 8192 + cg * 1024 : OOB);
                    if (r * 36 + idx == PUBI && w >= 2) { A2_PUBLISH() STAMP(4); }
                    if (r * 36 + idx == POLI) fa = LD16S(rws, lane * 16, OFF_FL + FL_A2 + xc * 1024);
                    if (r > A2R && ((r - A2R - 1) * 36 + idx) % (((r - A2R - 1) * 36 + idx) < 24 ? 2 : WCD) == 1) {
                        const int t = (r - A2R - 1) * 36 + idx, n = t < 24 ? t / 2 : 12 + (t - 24) / WCD;
                        if (n < 4) x0[n] = LD8S(rws, tk * 2 + n * 512, OFF_X0T + ch * 4096);
                        else if (n < 8) sa[n - 4] = __builtin_bit_cast(f32x4, LD16S(rws, tk * 4 + (n - 4) * 1024, OFF_ST0));
                        else if (n < 12) sb[n - 8] = __builtin_bit_cast(f32x4, LD16S(rws, tk * 4 + (n - 8) * 1024, OFF_ST0 + 8192));
                        else if (n - 12 < Q_CONV) WSEQ(n - 12)
                    }
                    SB();
                    ++idx;
                }
            if (r == A2R) {
                POLL_ALL(fa, OFF_FL + FL_A2 + xc * 1024);
                STAMP(7);
            }
        };
        conv_step(IC<0>{});
        conv_step(IC<1>{});
        conv_step(IC<2>{});
        conv_step(IC<3>{});
        MF_DONE();
#pragma unroll
        for (int i = 0; i < 8; ++i) PIN_V(acc[i]);
        STAMP(8);
        LAS f32x4* xch = (LAS f32x4*)(smem + CV_X);
        auto conv_out = [&](auto hb_) __attribute__((always_inline)) {
            constexpr int hb = decltype(hb_)::value;
#pragma unroll
            for (int i = 0; i < 4; ++i) xch[(w * 4 + i) * 64 + lane] = acc[4 - 4 * hb + i];
            SB();
            const float dval = lo2f(draw);
            f32x2 ya[4][2], xa[4][2], na[4][2];
#pragma unroll
            for (int i = 0; i < 4; ++i) {
                ya[i][0] = fma2(f32x2{hi2f(uu[i].y), lo2f(uu[i].y)}, f32x2{dval, dval}, acc[4 * hb + i].xy);
                ya[i][1] = fma2(f32x2{hi2f(uu[i].x), lo2f(uu[i].x)}, f32x2{dval, dval}, acc[4 * hb + i].zw);
                asm volatile("" : "+v"(ya[i][0]), "+v"(ya[i][1]));
            }
            BAR();
            f32x4 oth[4];
#pragma unroll
            for (int i = 0; i < 4; ++i) oth[i] = xch[((w ^ 1) * 4 + i) * 64 + lane];
            SB();
#pragma unroll
            for (int i = 0; i < 4; ++i) {
                xa[i][0] = up2(x0[i].x);
                xa[i][1] = up2(x0[i].y);
                na[i][0] = fma2(xa[i][0], sa[i].xy, sb[i].xy);
                na[i][1] = fma2(xa[i][1], sa[i].zw, sb[i].zw);
            }
            asm volatile("" : "+v"(na[0][0]), "+v"(na[0][1]), "+v"(na[1][0]), "+v"(na[1][1]), "+v"(na[2][0]), "+v"(na[2][1]), "+v"(na[3][0]), "+v"(na[3][1]));
            const int off = (32 * hb + (c >> 1)) * 32768 + ch * 64 + (((c & 1) ^ ((ch >> 2) & 1)) << 5) + 8 * g;
#pragma unroll
            for (int i = 0; i < 4; ++i) {
                ya[i][0] += oth[i].xy;
                ya[i][1] += oth[i].zw;
                ST8S((v2u{pk2(fma2(na[i][0], ya[i][0], xa[i][0])), pk2(fma2(na[i][1], ya[i][1], xa[i][1]))}), rws, off, OFF_X1 + i * 8 * 32768);
            }
            SB();
#pragma unroll
            for (int i = 0; i < 4; ++i) ST8S((v2u{pk2(ya[i][0]), pk2(ya[i][1])}), rws, off, OFF_Y + i * 8 * 32768);
            SB();
        };
        if (half) conv_out(IC<1>{}); else conv_out(IC<0>{});
        STAMP(9);
        const int fb1 = tid < 8 ? tid * 1024 + b * 4 : OOB;
#pragma unroll
        for (int n = Q_CONV; n < Q_Y; ++n) WSEQ(n)
        VMW(Q_Y - Q_CONV);
        BAR();
        FLAG(fb1, OFF_FL + FL_B);
        STAMP(10);
        __builtin_amdgcn_s_sleep(1);
        glds8(ws + w1o, lane * 16, (u32)(size_t)smem + L_W + w * 8192);
#pragma unroll
        for (int n = Q_Y; n < Q_B; ++n) WSEQ(n)
        const v4u fb = LD16S(rws, lane * 16, OFF_FL + FL_B + xc * 1024);
        SB();
        {
            v4u ones = {0x3f803f80u, 0x3f803f80u, 0x3f803f80u, 0x3f803f80u};
            PIN_V(ones);
            asm volatile("s_nop 1");
            f32x4 cs[3];
#pragma unroll
            for (int n = 0; n < NCS; ++n) {
                if (n == (Q_Y < NCS ? Q_Y : NCS - 1)) {
                    VMW(Q_B - Q_Y + 1);
                    v4u wl[8];
#pragma unroll
                    for (int m = 0; m < 8; ++m) wl[m] = *(const LAS v4u*)(smem + L_W + w * 8192 + m * 1024 + lane * 16);
                    SB();
#pragma unroll
                    for (int m = 0; m < 8; ++m) MF_(cs[m % 3], "v", wl[m], "v", ones);
                }
                if (n < NA1) MF(n < 3, cs[(n + 8) % 3], "a", W1[n], "v", ones);
                else MF(n < 3, cs[(n + 8) % 3], "v", W1[n], "v", ones);
                SB();
            }
            MF_DONE();
#pragma unroll
            for (int j = 0; j < 3; ++j) {
                PIN_V(cs[j]);
                *(LAS f32x4*)(smem + L_CS + (w * 3 + j) * 64 + g * 16) = cs[j];
            }
        }
        STAMP(11);
        POLL_ALL(fb, OFF_FL + FL_B + xc * 1024);
        STAMP(12);
        const u32 ldsb = (u32)(size_t)smem + w * 8192;
        glds8(ws + OFF_X1 + grp * 65536 + w * 8192, lane * 16, ldsb + L_X);
        FLAG(b == 0 && tid == 0 ? 0 : OOB, OFF_FL + FL_EPOCH);
#pragma unroll
        for (int n = Q_B; n < Q_X; ++n) WSEQ(n)
        VMW(1 + Q_X - Q_B);
    }
    v4u H[HR][2];
    u32 xq[2][2][2];
    v2u yq[2][2];
    f32x4 ps[2];
    auto ffn = [&](auto layer_) __attribute__((always_inline)) {
        constexpr int layer = decltype(layer_)::value;
        int bl = blockIdx.x, w = __builtin_amdgcn_readfirstlane(tid >> 6);
        asm volatile("" : "+s"(bl), "+s"(w));
        const int grp = 4 * (bl & 7) + (bl >> 6), cg = (bl >> 3) & 7;
        const int T0 = 11 * cg + 3 * w, Tend = cg == 7 ? NT : 11 * cg + 11;
        const u32 ldsb = (u32)(size_t)smem + w * 8192 + 4096;
        const char* gb = ws + grp * 65536 + w * 8192 + 4096;
        const rsrc_t rh = mkrs(ws + OFF_H + grp * (2 * NP * 2048), 2 * NP * 2048);
        int lane = tid & 63;
        PIN_V(lane);
        const int c = lane & 15, g = lane >> 4;
        const int w2o = OFF_W2T + (cg * NP + w) * 4096, w2v = lane * 16 + 2048 * (w >> 1);
        const int w1o = OFF_W1T + (cg * 4 + w) * 49152;
        int w1v[3];
#pragma unroll
        for (int j = 0; j < 3; ++j) w1v[j] = T0 + j < Tend ? j * 1024 + lane * 16 : OOB;
        const int hv0 = (w * 2 + (w & 1)) * 1024 + lane * 16, hv1 = hv0 ^ 1024;
        const int fgo = OFF_FL + FL_G + grp * 1024, fvo = (lane & 7) * 128, fsv = tid == 0 ? cg * 128 : OOB;
        const int tt = w & 1, chl = 32 * (w >> 1) + 2 * c;
        const int xo = (64 * cg + chl) * 64 + ((tt ^ ((c >> 1) & 1)) << 5) + 8 * g;
        u32 fl;
#define H_LOAD1(zz, i) H[(zz) % HR][i] = LD16S(rh, (zz) % 11 == 10 && w == 3 ? OOB : ((i) ? hv1 : hv0), (zz) / 11 * NP * 2048 + (zz) % 11 * 8192);
#define W2_LATE(zz)                                                                          \
    if ((zz) < 11 && 4 * (zz) + 44 > Q_H && layer == 0) {                                    \
        _Pragma("unroll") for (int j = 0; j < 4; ++j)                                        \
            if (4 * (zz) + j + 40 >= Q_H) W2_LOAD((zz) < 11 ? (zz) : 0, j);   \
    }
#define H_LOAD(zz) { W2_LATE(zz) H_LOAD1(zz, 0) H_LOAD1(zz, 1) }
#define H_B3(p, q, r) __builtin_amdgcn_bitop3_b32(p, q, r, 0x80)
#define H_BF(zz, p, q) __builtin_amdgcn_bitop3_b32(p, q, (zz) % 11 == 10 && w == 3 ? 0u : 0x80008000u, 0x2a)
#define H_BAD(zz)                                                                              \
    H_BF(zz, H_B3(H[(zz) % HR][0].w, H[(zz) % HR][1].x, H[(zz) % HR][1].y),                    \
         H_B3(H[(zz) % HR][1].z, H[(zz) % HR][1].w, H_B3(H[(zz) % HR][0].x, H[(zz) % HR][0].y, H[(zz) % HR][0].z)))
#define H_RETRY(zz)                                                         \
    {                                                                       \
        u32 spins_ = 0;                                                     \
        while (!dead) {                                                     \
            __builtin_amdgcn_s_sleep(2);                                    \
            asm volatile("" ::: "memory");                                  \
            H_LOAD1(zz, 0) H_LOAD1(zz, 1)                                   \
            if (!__any(H_BAD(zz) != 0u)) break;                             \
            if (++spins_ > SPIN_CAP) dead = true;                           \
        }                                                                   \
    }
#define H_CHECK(zz) if (__builtin_expect(__any(H_BAD(zz) != 0u), 0)) H_RETRY(zz)
#define PS_LOAD(hf)                                                                                         \
    _Pragma("unroll") for (int e = 0; e < 2; ++e)                                                           \
        ps[e] = __builtin_bit_cast(f32x4, LD16S(rws, c * 256 + (16 * tt + 4 * g) * 8 + e * 16, OFF_ST2 + (2 * grp + (hf)) * 4096));
#define PS_SUM()                                                                                                    \
    _Pragma("unroll") for (int e = 0; e < 8; ++e) ps[e >> 2][e & 3] = dppadd<0x121>(ps[e >> 2][e & 3]);             \
    _Pragma("unroll") for (int e = 0; e < 8; ++e) ps[e >> 2][e & 3] = dppadd<0x122>(ps[e >> 2][e & 3]);             \
    _Pragma("unroll") for (int e = 0; e < 8; ++e) ps[e >> 2][e & 3] = dppadd<0x124>(ps[e >> 2][e & 3]);             \
    _Pragma("unroll") for (int e = 0; e < 8; ++e) ps[e >> 2][e & 3] = dppadd<0x128>(ps[e >> 2][e & 3]);
#define PS_NAN() __any(!((ps[0].y + ps[0].w) + (ps[1].y + ps[1].w) + ((ps[0].x + ps[0].z) + (ps[1].x + ps[1].z)) ==  \
                         (ps[0].y + ps[0].w) + (ps[1].y + ps[1].w) + ((ps[0].x + ps[0].z) + (ps[1].x + ps[1].z))))
#define X3_PUBLISH(hf)                                                                                              \
    {                                                                                                               \
        PS_SUM()                                                                                                    \
        if (__builtin_expect(PS_NAN(), 0)) {                                                                        \
            u32 spins_ = 0;                                                                                         \
            while (!dead) {                                                                                         \
                __builtin_amdgcn_s_sleep(2);                                                                        \
                asm volatile("" ::: "memory");                                                                      \
                PS_LOAD(hf)                                                                                         \
                PS_SUM()                                                                                            \
                if (!PS_NAN()) break;                                                                               \
                if (++spins_ > (SPIN_CAP >> 2)) dead = true;                                                        \
            }                                                                                                       \
        }                                                                                                           \
        f32x2 a_[2], b_[2];                                                                                         \
        _Pragma("unroll") for (int e = 0; e < 2; ++e) {                                                             \
            const f32x2 mean_ = f32x2{ps[e].x, ps[e].z} * (1.f / 512.f);                                            \
            const f32x2 var_ = f32x2{ps[e].y, ps[e].w} * (1.f / 512.f) - mean_ * mean_;                             \
            a_[e] = f32x2{__builtin_amdgcn_rsqf(fmaxf(var_.x, 0.f) + 1e-5f), __builtin_amdgcn_rsqf(fmaxf(var_.y, 0.f) + 1e-5f)}; \
            b_[e] = -mean_ * a_[e];                                                                                 \
        }                                                                                                           \
        _Pragma("unroll") for (int j = 0; j < 2; ++j) {                                                             \
            const f32x2 x0_ = up2(xq[hf][j][0]), x1_ = up2(xq[hf][j][1]);                                           \
            ST8S((v2u{pk2(fma2(fma2(x0_, a_[0], b_[0]), up2(yq[hf][j].x), x0_)), pk2(fma2(fma2(x1_, a_[1], b_[1]), up2(yq[hf][j].y), x1_))}), \
                 rws, xo + 64 * j, OFF_X2 + (2 * grp + (hf)) * 32768);                                              \
        }                                                                                                           \
    }
#define X_PIECE(hf, n) glds1x(n, gb + (layer ? OFF_X2 : OFF_X1) + (hf) * 32768, lane * 16, ldsb + L_X + (hf) * 32768);
        auto gemm1 = [&](auto hf_) __attribute__((always_inline)) {
            constexpr int hf = decltype(hf_)::value;
            STAMP(16 + layer * 24 + hf * 5);
            const LAS char* xb0[2];
#pragma unroll
            for (int i = 0; i < 2; ++i) xb0[i] = smem + L_X + hf * 32768 + (4 * g + (c >> 2)) * 64 + (((i ^ w ^ g) & 1) << 5) + 8 * (c & 3);
            v4u X[3][2], WL[8];
            f32x4 acc[3][2], accS, accG;
            v4u ones = {0x3f803f80u, 0x3f803f80u, 0x3f803f80u, 0x3f803f80u};
            PIN_V(ones);
#define G1_LOAD(kk, i)                                                                                          \
    {                                                                                                           \
        const LAS char* p_ = xb0[i] + ((((kk) >> 2) + w) & 3) * 8192 + ((kk) & 3) * 2048;                       \
        const v2u lo = tr_read(p_), hi = tr_read(p_ + 1024);                                                    \
        X[(kk) % 3][i] = v4u{lo.x, lo.y, hi.x, hi.y};                                                           \
    }
#define G1_LOADH(kk, i, hh)                                                                                      \
    {                                                                                                           \
        const v2u t_ = tr_read(xb0[i] + ((((kk) >> 2) + w) & 3) * 8192 + ((kk) & 3) * 2048 + (hh) * 1024);      \
        X[(kk) % 3][i][2 * (hh)] = t_.x;                                                                        \
        X[(kk) % 3][i][2 * (hh) + 1] = t_.y;                                                                    \
    }
#define WL_LOAD(n) WL[n] = *(const LAS v4u*)(smem + L_W + w * 8192 + (n) * 1024 + lane * 16);
#define G1_MF(kk, j, i)                                                                                          \
    {                                                                                                            \
        const int n_ = (kk) * 3 + (j) - 8;                                                                       \
        if (n_ < 0) MF((kk) == 0, acc[j][i], "v", WL[n_ < 0 ? n_ + 8 : 0], "v", X[(kk) % 3][i]);                 \
        else if (n_ < NA1) MF_(acc[j][i], "a", W1[n_ < 0 ? 0 : n_], "v", X[(kk) % 3][i]);                        \
        else MF_(acc[j][i], "v", W1[n_ < 0 ? 0 : n_], "v", X[(kk) % 3][i]);                                      \
    }
            WL_LOAD(0) WL_LOAD(1) WL_LOAD(2) WL_LOAD(3) WL_LOAD(4) WL_LOAD(5) WL_LOAD(6) WL_LOAD(7)
            if (hf == 1) VMW(6 + (layer ? 0 : Q_E - Q_A));
            if (hf == 0 && layer == 1) VMW(2);
            STAMP(17 + layer * 24 + hf * 5);
            auto head = [&]() __attribute__((always_inline)) -> bool {
                G1_LOAD(0, 1) G1_LOAD(0, 0) G1_LOAD(1, 1) G1_LOAD(1, 0)
                v4u xs[8];
                u32 mk = 0;
                if (layer == 1) {
#pragma unroll
                    for (int i = 0; i < 8; ++i) xs[i] = *(const LAS v4u*)(smem + L_X + hf * 32768 + w * 8192 + lane * 16 + i * 1024);
                }
                SB();
#pragma unroll
                for (int n = 0; n < 16; ++n) {
                    const int kk = n >> 3, m = n & 7;
                    if (m < 3) G1_MF(kk, m % 3, 0)
                    else if (m == 3) MF(kk == 0, accS, "v", ones, "v", X[kk][0]);
                    else if (m == 4) MF(kk == 0, accG, "v", X[kk][0], "v", X[kk][0]);
                    else G1_MF(kk, (m + 1) % 3, 1)
                    if (m < 4) G1_LOADH(kk + 2, 1 - (m >> 1), m & 1)
                    if (layer == 1) mk = max(max(mk, xs[n >> 1][2 * (n & 1)]), xs[n >> 1][2 * (n & 1) + 1]);
                    if (layer == 1 && (n & 1)) PIN_V(mk);
                    if (layer == 0 && hf == 0 && m == 5) X_PIECE(1, kk)
                    SB();
                }
                return layer == 1 && __any(mk == ~0u);
            };
            if (__builtin_expect(head(), 0)) {
                for (u32 spins = 0; !dead;) {
                    if (++spins > (SPIN_CAP >> 3)) dead = true;
                    asm volatile("s_waitcnt lgkmcnt(0)" ::: "memory");
#pragma unroll
                    for (int n = 0; n < 8; ++n) X_PIECE(hf, n)
                    VMW(0);
                    if (!head()) break;
                }
            }
            STAMP(18 + layer * 24 + hf * 5);
            LAS f32x2* ls = (LAS f32x2*)(smem + L_ST + hf * 512);
            f32x2 s2[2][2];
            f32x4 ct[3];
            f32x2 mv[2], rs2, nb2;
#pragma unroll
            for (int kk = 2; kk < 16; ++kk) {
                if (kk == 2) BAR();
                if (layer == 0 && hf == 1 && kk == FKK) {
                    constexpr int PER = (Q_H - Q_E + WSTEPS - 1) / WSTEPS, NY = Q_E - Q_A + ((FKK - 2) * PER < Q_H - Q_E ? (FKK - 2) * PER : Q_H - Q_E);
                    VMW(NY);
                    BAR();
                    FLAG(fsv, fgo);
                }
                G1_MF(kk, 0, 0)
                if (kk < 14) G1_LOADH(kk + 2, 1, 0)
                if (kk == 14) { rs2 = f32x2{__builtin_amdgcn_rsqf(rs2.x), __builtin_amdgcn_rsqf(rs2.y)}; PIN_V(rs2); }
                SB();
                G1_MF(kk, 1, 0)
                if (kk < 14) G1_LOADH(kk + 2, 1, 1)
                if (kk == 14) { nb2 = f32x2{mv[0].x, mv[1].x} * (layer ? rs2 : -rs2); if (layer) rs2 = -rs2; asm volatile("" : "+v"(rs2), "+v"(nb2)); }
                SB();
                G1_MF(kk, 2, 0)
                if (kk < 14) G1_LOADH(kk + 2, 0, 0)
                SB();
                if (kk < 8) {
                    MF_(accS, "v", ones, "v", X[kk % 3][0]);
                    MF_(accG, "v", X[kk % 3][0], "v", X[kk % 3][0]);
                }
                if (layer == 1 && hf == 0 && kk == PKK) X3_PUBLISH(1)
                G1_MF(kk, 0, 1)
                if (kk < 14) G1_LOADH(kk + 2, 0, 1)
                SB();
                G1_MF(kk, 1, 1)
                if (layer == 0 && hf == 0 && kk < 8) X_PIECE(1, kk)
                if (layer == 1 && hf == 0 && kk >= XKK && kk < XKK + 4) X_PIECE(1, 2 * (kk - XKK))
                if (layer == 0 && hf == 1 && kk == CPOLL) fl = LD4S(fvo, fgo);
                if (layer == 1 && hf == 1 && kk >= 16 - HR) H_LOAD1(kk - (16 - HR), 0)
                if (kk == 8) {
                    asm volatile("" : "+v"(accS), "+v"(accG));
                    const float g0 = accG.x, g1 = accG.y, g2 = accG.z, g3 = accG.w;
                    mv[0] = f32x2{c & 1 ? g1 : g0, c & 1 ? g3 : g2};
                    PIN_V(mv[0]);
                }
                if (kk == 9) ls[g == (c >> 2) ? w * 16 + c : 128 + lane] = f32x2{accS.x, c & 2 ? mv[0].y : mv[0].x};
                if (kk == 11) {
#pragma unroll
                    for (int i = 0; i < 2; ++i) {
                        s2[i][0] = ls[((i ^ w) & 1) * 16 + c];
                        s2[i][1] = ls[(((i ^ w) & 1) + 2) * 16 + c];
                    }
                }
                if (kk == 12) {
#pragma unroll
                    for (int j = 0; j < 3; ++j) ct[j] = *(const LAS f32x4*)(smem + L_CS + (w * 3 + j) * 64 + g * 16);
                }
                if (kk == 13) {
#pragma unroll
                    for (int i = 0; i < 2; ++i) mv[i] = (s2[i][0] + s2[i][1]) * (1.f / 512.f);
                    asm volatile("" : "+v"(mv[0]), "+v"(mv[1]));
                }
                SB();
                G1_MF(kk, 2, 1)
                if (layer == 1 && hf == 0 && kk >= XKK && kk < XKK + 4) X_PIECE(1, 2 * (kk - XKK) + 1)
                if (layer == 1 && hf == 1 && kk >= 16 - HR) H_LOAD1(kk - (16 - HR), 1)
                if (kk == 13) {
                    rs2 = f32x2{fmaxf(mv[0].y - mv[0].x * mv[0].x, 0.f) + 1e-5f, fmaxf(mv[1].y - mv[1].x * mv[1].x, 0.f) + 1e-5f};
                    PIN_V(rs2);
                }
                if (layer == 0) {
                    constexpr int N0 = hf ? Q_E : Q_X, N1 = hf ? Q_H : Q_A, PER = (N1 - N0 + WSTEPS - 1) / WSTEPS;
#pragma unroll
                    for (int e = 0; e < 6; ++e) {
                        const int n = N0 + (kk - 2) * PER + e;
                        if (e < PER && n < N1) WSEQ(n)
                    }
                }
                if (kk == 10) BAR();
                SB();
            }
            const float rs[2] = {rs2.x, rs2.y}, nb[2] = {nb2.x, nb2.y};
            STAMP(19 + layer * 24 + hf * 5);
            if (hf == 0 && NCS < 40 && layer == 0) {
#pragma unroll
                for (int n = NCS; n < 40; ++n) {
                    if (n < NA1) MF_(ct[(n + 8) % 3], "a", W1[n], "v", ones);
                    else MF_(ct[(n + 8) % 3], "v", W1[n], "v", ones);
                }
                MF_DONE();
#pragma unroll
                for (int j = 0; j < 3; ++j) {
                    PIN_V(ct[j]);
                    *(LAS f32x4*)(smem + L_CS + (w * 3 + j) * 64 + g * 16) = ct[j];
                }
            }
            if (layer == 0 && hf == 1) {
                POLL_FINISH(fl, fvo, fgo);
#pragma unroll
                for (int z = 0; z < NPRE; ++z) H_LOAD(z)
                SB();
            }
#pragma unroll
            for (int i = 0; i < 2; ++i) {
                f32x4 u[3];
#pragma unroll
                for (int j = 0; j < 3; ++j) u[j] = ct[j] * nb[i];
                asm volatile("" : "+v"(u[0]), "+v"(u[1]), "+v"(u[2]));
                if (i == 0) MF_DONE();
                asm volatile("" : "+v"(acc[0][i]), "+v"(acc[1][i]), "+v"(acc[2][i]));
#pragma unroll
                for (int j = 0; j < 3; ++j) u[j] = acc[j][i] * rs[i] + u[j];
                v2u o[3];
#pragma unroll
                for (int j = 0; j < 3; ++j) o[j] = v2u{pk2(u[j].xy), pk2(u[j].zw)};
                asm volatile("" : "+v"(o[0]), "+v"(o[1]), "+v"(o[2]));
#pragma unroll
                for (int j = 0; j < 3; ++j) {
                    const int T = T0 + j;
                    ST8S((v2u{relu2<layer>(o[j].x), relu2<layer>(o[j].y)}), rh,
                         T < Tend ? (((i ^ w) & 1) * 1024 + (g >> 1) * 256 + c * 16 + (g & 1) * 8) : OOB,
                         hf * NP * 2048 + (T >> 1) * 2048 + 512 * (T & 1));
                }
                SB();
            }
            if (layer == 0 && hf == 0) {
#pragma unroll
                for (int n = Q_A; n < Q_E; ++n) WSEQ(n)
                SB();
            }
            if (layer == 0 && hf == 1) {
#pragma unroll
                for (int z = NPRE; z < HR; ++z) H_LOAD(z)
                SB();
            }
            STAMP(20 + layer * 24 + hf * 5);
        };
        gemm1(IC<0>{});
        gemm1(IC<1>{});
        if (layer == 1) { H_CHECK(0) }
        SB();
        STAMP(26 + layer * 24);
        auto gemm2 = [&](auto hf_) __attribute__((always_inline)) {
            constexpr int hf = decltype(hf_)::value;
            f32x4 acc[2][4];
            v2u res[2];
            LAS f32x4* red = (LAS f32x4*)(smem + L_RED);
#define RED_PUT(n) { PIN_V(acc[(n) >> 2][(n) & 3]); red[(((w ^ (((n) >> 2) + 2 * (((n) & 3) >> 1))) * 3 + ((n) >> 2) + 2 * (((n) & 3) >> 1) - 1) * 2 + ((n) & 1)) * 64 + lane] = acc[(n) >> 2][(n) & 3]; SB(); }
#pragma unroll
            for (int s = 0; s < 11; ++s) {
                const int z = 11 * hf + s;
                const bool chk = layer == 1 && s < 10, more = layer == 1 ? z + HR < 22 : s + HR < 11;
#define G2_MF(i, j) MF(s == 0, acc[i][j], "v", H[z % HR][i], "a", W2[s][j]);
                u32 b0 = 0, b1 = 0;
                G2_MF(1, 3)
                G2_MF(1, 2)
                if (chk) { b0 = H_B3(H[(z + 1) % HR][0].x, H[(z + 1) % HR][0].y, H[(z + 1) % HR][0].z); PIN_V(b0); }
                G2_MF(1, 1)
                if (chk) {
                    b1 = H_B3(H[(z + 1) % HR][0].w, H[(z + 1) % HR][1].x, H[(z + 1) % HR][1].y);
                    b0 = H_B3(H[(z + 1) % HR][1].z, H[(z + 1) % HR][1].w, b0);
                    asm volatile("" : "+v"(b0), "+v"(b1));
                }
                G2_MF(1, 0)
                bool bad = false;
                if (chk) bad = __any(H_BF(z + 1, b0, b1) != 0u);
                SB();
                G2_MF(0, 3)
                if (more) { W2_LATE(z + HR) H_LOAD1(z + HR, 1) }
                SB();
                G2_MF(0, 2)
                SB();
                if (chk) { if (__builtin_expect(bad, 0)) H_RETRY(z + 1) }
                if (s == 10) { asm volatile("s_nop 2" ::: "memory"); RED_PUT(7) }
                G2_MF(0, 1)
                if (s == 10) RED_PUT(6)
                G2_MF(0, 0)
                SB();
                if (s == (layer == 0 && hf == 0 ? NPRE : 4) && (hf == 1 || layer == 0)) {
                    BAR();
                    if (hf == 0) FLAG(fsv, fgo + 32768);
                }
                if (more) H_LOAD1(z + HR, 0)
                if (layer == 0 && hf == 0 && s == CPOLL2) fl = LD4S(fvo, fgo + 32768);
                if (layer == 0 && s == 5) {
#pragma unroll
                    for (int j = 0; j < 2; ++j) yq[hf][j] = LD8S(rws, xo + 64 * j, OFF_Y + (2 * grp + hf) * 32768);
                }
                if (layer == 0 && hf == 1 && s == SQ) PS_LOAD(0)
                if (s == 8) {
                    res[0] = *(const LAS v2u*)(smem + L_X + hf * 32768 + xo);
                    res[1] = *(const LAS v2u*)(smem + L_X + hf * 32768 + xo + 64);
                }
                SB();
            }
            STAMP(27 + layer * 24 + hf * 4);
            RED_PUT(5) RED_PUT(4) RED_PUT(3) RED_PUT(2)
            if (layer == 0 && hf == 1) { X3_PUBLISH(0) SB(); }
            f32x2 xf[2][2];
            asm volatile("s_nop 1" : "+v"(acc[0][0]), "+v"(acc[0][1]));
#pragma unroll
            for (int j = 0; j < 2; ++j) {
                xf[j][0] = layer ? up2(res[j].x) - acc[0][j].xy : up2(res[j].x) + acc[0][j].xy;
                xf[j][1] = layer ? up2(res[j].y) - acc[0][j].zw : up2(res[j].y) + acc[0][j].zw;
            }
            BAR();
            f32x4 rd[6];
#pragma unroll
            for (int i = 0; i < 6; ++i) rd[i] = red[(w * 6 + i) * 64 + lane];
            SB();
            STAMP(28 + layer * 24 + hf * 4);
            if (layer == 0 && hf == 0) {
                POLL_FINISH(fl, fvo, fgo + 32768);
#pragma unroll
                for (int z = 11; z < 11 + NPRE2; ++z) H_LOAD(z)
                SB();
            }
#pragma unroll
            for (int j = 0; j < 2; ++j) {
                const f32x4 t = rd[j] + (rd[2 + j] + rd[4 + j]);
                xf[j][0] = layer ? xf[j][0] - t.xy : xf[j][0] + t.xy;
                xf[j][1] = layer ? xf[j][1] - t.zw : xf[j][1] + t.zw;
            }
            STAMP(29 + layer * 24 + hf * 4);
            if (layer == 1) {
#pragma unroll
                for (int r = 0; r < 4; ++r)
                    __builtin_amdgcn_raw_buffer_store_b32(pk2bf(xf[0][r >> 1][r & 1], xf[1][r >> 1][r & 1]), mkrs(out, 2 * MB),
                                                          (16 * tt + 4 * g + r) * 1024 + chl * 2, (64 * grp + 32 * hf) * 1024 + cg * 128, 0);
            } else {
#pragma unroll
                for (int j = 0; j < 2; ++j) {
                    xq[hf][j][0] = pk2(xf[j][0]);
                    xq[hf][j][1] = pk2(xf[j][1]);
                }
                f32x2 sm[2], sq[2];
#pragma unroll
                for (int r = 0; r < 2; ++r) {
                    const f32x2 x0 = up2(xq[hf][0][r]), x1 = up2(xq[hf][1][r]);
                    sm[r] = x0 + x1;
                    sq[r] = fma2(x0, x0, x1 * x1);
                }
                f32x4 s4;
                {
                    const bool odd = c & 1;
                    const f32x4 keep = odd ? f32x4{sm[1].x, sq[1].x, sm[1].y, sq[1].y} : f32x4{sm[0].x, sq[0].x, sm[0].y, sq[0].y};
                    const f32x4 give = odd ? f32x4{sm[0].x, sq[0].x, sm[0].y, sq[0].y} : f32x4{sm[1].x, sq[1].x, sm[1].y, sq[1].y};
#pragma unroll
                    for (int e = 0; e < 4; ++e) {
                        const float gv = give[e];
                        s4[e] = keep[e] + __builtin_bit_cast(float, __builtin_amdgcn_update_dpp(0, __builtin_bit_cast(int, gv), 0xb1, 0xf, 0xf, true));
                    }
#pragma unroll
                    for (int e = 0; e < 4; ++e) s4[e] = dppadd<0x122>(s4[e]);
#pragma unroll
                    for (int e = 0; e < 4; ++e) s4[e] = dppadd<0x124>(s4[e]);
#pragma unroll
                    for (int e = 0; e < 4; ++e) s4[e] = dppadd<0x128>(s4[e]);
                }
                ST16S(__builtin_bit_cast(v4u, s4), rws, c < 2 ? (cg * 2 + (w >> 1)) * 256 + (16 * tt + 4 * g + 2 * c) * 8 : OOB,
                      OFF_ST2 + (2 * grp + hf) * 4096);
            }
            SB();
            if (hf == 0 && layer == 0) {
#pragma unroll
                for (int z = 11 + NPRE2; z < 11 + HR; ++z) H_LOAD(z)
                SB();
            }
            if (hf == 0 && layer == 1) { H_CHECK(11) }
            if (hf == 1 && layer == 0) {
                __builtin_amdgcn_s_sleep(XSLEEP);
#pragma unroll
                for (int n = 0; n < 8; ++n) glds1x(n, gb + OFF_X2, lane * 16, ldsb + L_X);
                __builtin_amdgcn_s_sleep(PSLEEP2);
                asm volatile("" ::: "memory");
                PS_LOAD(1)
                SB();
            }
            STAMP(30 + layer * 24 + hf * 4);
        };
        gemm2(IC<0>{});
        gemm2(IC<1>{});
    };
    ffn(IC<0>{});
    ffn(IC<1>{});
    asm volatile(".Lpf_end:");
}

extern "C" void kernel_launch(void* const* d_in, const int* in_sizes, int n_in, void* d_out, int out_size, void* d_ws,
                              size_t ws_size, hipStream_t stream) {
    constexpr int lds = L_BYTES;
    (void)hipFuncSetAttribute((const void*)k_all, hipFuncAttributeMaxDynamicSharedMemorySize, lds);
    k_all<<<256, 256, lds, stream>>>((const u16*)d_in[0], (const u16*)d_in[1], (const u16*)d_in[2], (const u16*)d_in[3],
                                     (const u16*)d_in[4], (const u16*)d_in[5], (const u16*)d_in[7], (const u16*)d_in[8],
                                     (u16*)d_out, (char*)d_ws);
}
```

```cpp
#include <hip/hip_runtime.h>
#include <hip/hip_bf16.h>
#include <stdint.h>

typedef unsigned short u16;
typedef unsigned int u32;
typedef unsigned long long u64;
typedef u32 v4u __attribute__((ext_vector_type(4)));
typedef u32 v2u __attribute__((ext_vector_type(2)));
typedef float f32x4 __attribute__((ext_vector_type(4)));
typedef float f32x2 __attribute__((ext_vector_type(2)));
typedef short bf16x8 __attribute__((ext_vector_type(8)));
typedef short s16x4 __attribute__((ext_vector_type(4)));
typedef short s16x2 __attribute__((ext_vector_type(2)));
typedef __bf16 bf16x2 __attribute__((ext_vector_type(2)));
#define LAS __attribute__((address_space(3)))
#define GAS __attribute__((address_space(1)))
#define DEV __device__ __forceinline__
#define RLX __ATOMIC_RELAXED, __HIP_MEMORY_SCOPE_AGENT
typedef __amdgpu_buffer_rsrc_t rsrc_t;

constexpr int S_ = 2048, D_ = 512, F_ = 1376;
constexpr int NP = 43;
constexpr int NT = 86;
constexpr int MB = 1 << 20;
constexpr int OFF_FL = 0, OFF_ST0 = 512 * 1024;
constexpr int OFF_UR = 1 * MB, OFF_X0T = 3 * MB, OFF_Y = 5 * MB, OFF_X1 = 7 * MB, OFF_W1T = 11 * MB,
              OFF_W2T = 13 * MB, OFF_H = 15 * MB, OFF_X2 = OFF_H + 64 * NP * 2048, OFF_ST2 = OFF_X2 + 2 * MB, WS_USED = 27 * MB;
static_assert(OFF_ST2 + 64 * 4096 - OFF_H <= 256 * 31 * 1024, "the marked region is 31 KiB per workgroup");
constexpr int FL_EPOCH = 0, FL_A = 4096, FL_A2 = 40960, FL_B = 49152, FL_W = 57344, FL_G = 81920;
constexpr int SC1 = 16, OOB = 0x40000000;
constexpr int L_X = 0, L_Y = 65536, L_W = 98304, L_RED = 131072, L_ST = 155648, L_MS = L_ST + 2560, L_CS = L_MS + 1024, L_BYTES = L_CS + 1024;
constexpr int CV_E = 0, CV_O = 4160, CV_U = 8320, CV_CH = 12928, CV_X = 2 * CV_CH, L_P0 = 49152;
constexpr u32 SPIN_CAP = 1u << 18;
#define NA1 20
#define HR 6
#define PSLEEP 2
#define SECMODE 0
#define QAUX 2
#define NTAUX 2
#define WAUX 2
#define A2R 1
#define A1DELAY 12
#define WPOS 2
#define NPRE 3
#define CPOLL 12
#define CPOLL2 8
#define DPOLL 5
#define DCHK 10
#define MIDMIX 3
#define CODEPF 1
#define QEARLY 1
#define SECDELAY 1
#define PUBI -1
#define POLI 20
#define EARLYDELAY 0
#define WSTEPS 13
#define NPRE2 4
#define DPOLL2 10
#define ENDINV 0
#define CLRPOS 1
#define FKK 6
#define XKK 8
#define PKK 3
#define SQ 8
#define XSLEEP 5
#define PSLEEP2 9
#define STAG 1
#define PACE 0
#define BPOLL 10
#define PUBR 1
#define TLBW 0
#define WSOFF 0
#define Q_CONV 0
#define Q_Y 0
#define Q_B 16
#define Q_X 24
#define Q_A 50
#define Q_E 50
#define Q_H 76
#define WCD 2
static_assert(Q_Y == 0 && Q_CONV == 0 && Q_Y < Q_B && Q_B <= Q_X && Q_X <= Q_A && Q_A <= Q_E && Q_E <= Q_H && Q_H <= 84 && Q_A >= 40, "order");
static_assert(24 + Q_CONV * WCD <= (3 - A2R) * 36, "slots");
constexpr bool late_ok() {
    const int per = (Q_A - Q_X + WSTEPS - 1) / WSTEPS;
    for (int n = Q_X; n < 40; ++n)
        if (2 + (n - Q_X) / per >= (n + 8) / 3) return false;
    return per <= 6 && (Q_H - Q_E + WSTEPS - 1) / WSTEPS <= 6 && WSTEPS <= 14;
}
static_assert(!PACE || (Q_B - Q_Y >= PACE && BPOLL < 8 + Q_B - Q_Y), "pace");
static_assert(late_ok(), "a fragment would be used before it is requested");
constexpr int NCS = Q_B < 40 ? Q_B : 40;
template <int N> struct IC { static constexpr int value = N; };
DEV float lo2f(u32 d) { return __builtin_bit_cast(float, d << 16); }
DEV float hi2f(u32 d) { return __builtin_bit_cast(float, d & 0xffff0000u); }
DEV f32x2 up2(u32 d) { return f32x2{lo2f(d), hi2f(d)}; }
DEV u32 pk2(f32x2 x) { return __builtin_bit_cast(u32, __builtin_convertvector(x, bf16x2)); }
DEV u32 pk2bf(float a, float b) { return pk2(f32x2{a, b}); }
DEV f32x2 fma2(f32x2 a, f32x2 b, f32x2 c) { return __builtin_elementwise_fma(a, b, c); }
DEV f32x4 mfma(v4u a, v4u b, f32x4 c) {
    return __builtin_amdgcn_mfma_f32_16x16x32_bf16(__builtin_bit_cast(bf16x8, a), __builtin_bit_cast(bf16x8, b), c, 0, 0, 0);
}
DEV v2u tr_read(const LAS char* p) {
    return __builtin_bit_cast(v2u, __builtin_amdgcn_ds_read_tr16_b64_v4i16((LAS s16x4*)p));
}
DEV float xadd16(float v) {
    auto r = __builtin_amdgcn_permlane16_swap(__builtin_bit_cast(u32, v), __builtin_bit_cast(u32, v), false, false);
    const u32 r0 = r[0], r1 = r[1];
    return __builtin_bit_cast(float, r0) + __builtin_bit_cast(float, r1);
}
DEV float xadd32(float v) {
    auto r = __builtin_amdgcn_permlane32_swap(__builtin_bit_cast(u32, v), __builtin_bit_cast(u32, v), false, false);
    const u32 r0 = r[0], r1 = r[1];
    return __builtin_bit_cast(float, r0) + __builtin_bit_cast(float, r1);
}
template <int CTRL> DEV float dppadd(float v) {
    return v + __builtin_bit_cast(float, __builtin_amdgcn_update_dpp(0, __builtin_bit_cast(int, v), CTRL, 0xf, 0xf, true));
}
#define DPP_ADD(r, ror) "v_add_f32_dpp " r ", " r ", " r " row_ror:" ror " row_mask:0xf bank_mask:0xf\n\t"
#define DPP8(ror) DPP_ADD("%0", ror) DPP_ADD("%1", ror) DPP_ADD("%2", ror) DPP_ADD("%3", ror) DPP_ADD("%4", ror) DPP_ADD("%5", ror) DPP_ADD("%6", ror) DPP_ADD("%7", ror)
#define DPP4(ror) DPP_ADD("%0", ror) DPP_ADD("%1", ror) DPP_ADD("%2", ror) DPP_ADD("%3", ror)
#define ROWSUM8(rounds, a, b)                                                                                   \
    {                                                                                                           \
        float e0 = a.x, e1 = a.y, e2 = a.z, e3 = a.w, e4 = b.x, e5 = b.y, e6 = b.z, e7 = b.w;                   \
        asm volatile("s_nop 1\n\t" rounds : "+v"(e0), "+v"(e1), "+v"(e2), "+v"(e3), "+v"(e4), "+v"(e5), "+v"(e6), "+v"(e7)); \
        a = f32x4{e0, e1, e2, e3};                                                                              \
        b = f32x4{e4, e5, e6, e7};                                                                              \
    }
DEV float row16sum(float v) {
    v = dppadd<0x121>(v);
    v = dppadd<0x122>(v);
    v = dppadd<0x124>(v);
    v = dppadd<0x128>(v);
    return v;
}
typedef unsigned short u16x2 __attribute__((ext_vector_type(2)));
template <int NEG> DEV u32 relu2(u32 v) {
    if (NEG) return __builtin_bit_cast(u32, __builtin_elementwise_max(__builtin_bit_cast(u16x2, v), u16x2{0x8000, 0x8000}));
    return __builtin_bit_cast(u32, __builtin_elementwise_max(__builtin_bit_cast(s16x2, v), s16x2{0, 0}));
}
DEV rsrc_t mkrs(const void* p, int bytes) { return __builtin_amdgcn_make_buffer_rsrc((void*)p, 0, bytes, 0x00020000); }
#define LD16(r, vo, so) __builtin_amdgcn_raw_buffer_load_b128(r, vo, so, QAUX)
#define LD16N(r, vo, so) __builtin_amdgcn_raw_buffer_load_b128(r, vo, so, NTAUX)
#define LD4(r, vo, so) __builtin_amdgcn_raw_buffer_load_b32(r, vo, so, WAUX)
#define LD16S(r, vo, so) __builtin_amdgcn_raw_buffer_load_b128(r, vo, so, SC1)
#define LD8S(r, vo, so) __builtin_amdgcn_raw_buffer_load_b64(r, vo, so, SC1)
#define ST16S(v, r, vo, so) __builtin_amdgcn_raw_buffer_store_b128(v, r, (vo) + (so), 0, SC1)
#define ST8S(v, r, vo, so) __builtin_amdgcn_raw_buffer_store_b64(v, r, vo, so, SC1)
#define SB() __builtin_amdgcn_sched_barrier(0)
#define MF_(acc, ca, a, cb, b) asm volatile("v_mfma_f32_16x16x32_bf16 %0, %1, %2, %0" : "+v"(acc) : ca(a), cb(b))
#define MF0_(acc, ca, a, cb, b) asm volatile("v_mfma_f32_16x16x32_bf16 %0, %1, %2, 0" : "=&v"(acc) : ca(a), cb(b))
#define MF(first, acc, ca, a, cb, b) do { if (first) MF0_(acc, ca, a, cb, b); else MF_(acc, ca, a, cb, b); } while (0)
#define MF_DONE() asm volatile("s_nop 7" ::: "memory")
#define PIN_A(x) asm volatile("" : "+a"(x))
#define PIN_V(x) asm volatile("" : "+v"(x))
#define BAR() do { SB(); asm volatile("s_waitcnt lgkmcnt(0)\n\ts_barrier" ::: "memory"); SB(); } while (0)
#define VMW(n) do { SB(); asm volatile("s_waitcnt vmcnt(%0)" ::"n"(n) : "memory"); SB(); } while (0)
DEV void glds8(const char* sbase, u32 voff, u32 lds) {
    u32 keep;
    asm volatile(
        "s_mov_b32 %0, m0\n\ts_mov_b32 m0, %3\n\ts_nop 0\n\t"
        "global_load_lds_dwordx4 %1, %2 offset:-4096 sc1\n\t"
        "global_load_lds_dwordx4 %1, %2 offset:-3072 sc1\n\t"
        "global_load_lds_dwordx4 %1, %2 offset:-2048 sc1\n\t"
        "global_load_lds_dwordx4 %1, %2 offset:-1024 sc1\n\t"
        "global_load_lds_dwordx4 %1, %2 sc1\n\t"
        "global_load_lds_dwordx4 %1, %2 offset:1024 sc1\n\t"
        "global_load_lds_dwordx4 %1, %2 offset:2048 sc1\n\t"
        "global_load_lds_dwordx4 %1, %2 offset:3072 sc1\n\t"
        "s_mov_b32 m0, %0"
        : "=&s"(keep) : "v"(voff), "s"(sbase + 4096), "s"(lds + 4096) : "memory");
}
DEV void glds4(const char* sbase, u32 voff, u32 lds) {
    u32 keep;
    asm volatile(
        "s_mov_b32 %0, m0\n\ts_mov_b32 m0, %3\n\ts_nop 0\n\t"
        "global_load_lds_dwordx4 %1, %2 sc1\n\t"
        "global_load_lds_dwordx4 %1, %2 offset:1024 sc1\n\t"
        "global_load_lds_dwordx4 %1, %2 offset:2048 sc1\n\t"
        "global_load_lds_dwordx4 %1, %2 offset:3072 sc1\n\t"
        "s_mov_b32 m0, %0"
        : "=&s"(keep) : "v"(voff), "s"(sbase), "s"(lds) : "memory");
}
template <int OFF> DEV void glds1(const char* sbase, u32 voff, u32 lds) {
    asm volatile("s_mov_b32 m0, %2\n\ts_nop 0\n\tglobal_load_lds_dwordx4 %0, %1 offset:%3 sc1" ::"v"(voff), "s"(sbase), "s"(lds), "n"(OFF) : "memory");
}
DEV void glds1x(int i, const char* sbase, u32 voff, u32 lds) {
    switch (i) {
        case 0: glds1<-4096>(sbase, voff, lds); break;
        case 1: glds1<-3072>(sbase, voff, lds); break;
        case 2: glds1<-2048>(sbase, voff, lds); break;
        case 3: glds1<-1024>(sbase, voff, lds); break;
        case 4: glds1<0>(sbase, voff, lds); break;
        case 5: glds1<1024>(sbase, voff, lds); break;
        case 6: glds1<2048>(sbase, voff, lds); break;
        default: glds1<3072>(sbase, voff, lds); break;
    }
}
template <int OFF> DEV void gld16(f32x4& d, const char* sbase, u32 voff) {
    asm volatile("global_load_dwordx4 %0, %1, %2 offset:%3 sc1" : "=v"(d) : "v"(voff), "s"(sbase), "n"(OFF) : "memory");
}
DEV u32 mktag(u32 e) { return (u32)__builtin_amdgcn_readfirstlane(e) * 1664525u + 1013904223u; }
#define LD4S(vo, so) __builtin_amdgcn_raw_buffer_load_b32(rws, vo, so, SC1)
#define FLAG(vo, so) __builtin_amdgcn_raw_buffer_store_b32(tag, rws, vo, so, SC1)
#define POLL_FINISH(f, vo, so)                                            \
    do {                                                                  \
        if (!__all(f == tag)) {                                           \
            u32 spins_ = 0;                                               \
            bool ok_ = f == tag;                                          \
            while (!dead) {                                               \
                __builtin_amdgcn_s_sleep(PSLEEP);                         \
                asm volatile("" ::: "memory");                            \
                const u32 g_ = LD4S(ok_ ? OOB : (vo), so);                \
                ok_ = ok_ || g_ == tag;                                   \
                if (__all(ok_)) break;                                    \
                if (++spins_ > SPIN_CAP) dead = true;                     \
            }                                                             \
        }                                                                 \
        __builtin_amdgcn_fence(__ATOMIC_ACQUIRE, "wavefront");            \
        SB();                                                             \
    } while (0)
#define ALLOK(P) __all(P.x == tag && P.y == tag && P.z == tag && P.w == tag)
#define POLL_ALL(f, so)                                                   \
    do {                                                                  \
        if (!ALLOK(f)) {                                                  \
            u32 spins_ = 0;                                               \
            while (!dead) {                                               \
                __builtin_amdgcn_s_sleep(8);                              \
                asm volatile("" ::: "memory");                            \
                const v4u g_ = LD16S(rws, lane * 16, so);                 \
                if (ALLOK(g_)) break;                                     \
                if (++spins_ > SPIN_CAP) dead = true;                     \
            }                                                             \
        }                                                                 \
        __builtin_amdgcn_fence(__ATOMIC_ACQUIRE, "wavefront");            \
        SB();                                                             \
    } while (0)
#define STAMP(i)

__global__ __launch_bounds__(256, 1) void k_all(const u16* __restrict__ q, const u16* __restrict__ k, const u16* __restrict__ v,
                                                const u16* __restrict__ sk, const u16* __restrict__ ssmk,
                                                const u16* __restrict__ Dv, const u16* __restrict__ w1,
                                                const u16* __restrict__ w2, u16* __restrict__ out, char* __restrict__ ws) {
    extern __shared__ __attribute__((aligned(16))) char smem_[];
    LAS char* smem = (LAS char*)smem_;
    const int b = blockIdx.x, tid = threadIdx.x, w = __builtin_amdgcn_readfirstlane(tid >> 6);
    const int xc = b & 7, jb = b >> 3;
    const rsrc_t rws = mkrs(ws, WS_USED);
    bool dead = false;
    STAMP(0);
    const int ch0 = 64 * xc + 2 * jb;
    const int grp = 4 * xc + (b >> 6), cg = jb & 7;
    const int T0 = 11 * cg + 3 * w, Tend = cg == 7 ? NT : 11 * cg + 11;
    u32 tag;
    v4u W1[40], W2[11][4];
    {
        const int lane = tid & 63, c = lane & 15, g = lane >> 4;
#define Q_LOADS()                                                                                              \
    _Pragma("unroll") for (int i = 0; i < 4; ++i) hq[i] = LD16(r2, (w < 2 ? lane * 16 : qvo) + i * 1024, 0);   \
    _Pragma("unroll") for (int i = 0; i < 4; ++i) xr[i] = LD16(r2, (8 * b + 4 * half + 4 + i) * 1024 + lane * 16 - (w < 2 ? 0 : 4096), 0);
#define W_LOADS()                                                                                              \
    _Pragma("unroll") for (int i = 0; i < 8; ++i)                                                              \
        wr[i] = __builtin_amdgcn_raw_buffer_load_b128(                                                         \
            rw, wvo, is1 ? (16 * (i & 1) + 4 * (i >> 1)) * F_ * 2 : (16 * ((i >> 1) & 1) + (i >> 2) + 8 * (i & 1)) * D_ * 2, WAUX);
        u32 pf;
        {
            u64 pc;
            u32 len;
            asm volatile("s_getpc_b64 %0\n.Lpf_base:\n\ts_mov_b32 %1, .Lpf_end-.Lpf_base" : "=s"(pc), "=s"(len));
            pf = __builtin_amdgcn_raw_buffer_load_b32(mkrs((const void*)pc, len), w == 3 && lane < 14 ? jb * 1792 + lane * 128 : OOB, 0, 0);
        }
        const int cw = w >> 1, half = w & 1, ch = ch0 + cw;
        u32 draw;
        {
            const int row = 2 * (tid >> 3), s0 = 64 * jb + row;
            const int vo = s0 * 1024 + xc * 128 + (lane & 7) * 16;
            const rsrc_t rk = mkrs(k, 2 * MB), rv = mkrs(v, 2 * MB);
            v4u kk[5], vv[2], sw[4];
#pragma unroll
            for (int t = 0; t < 5; ++t) kk[t] = LD16N(rk, s0 + t - 3 >= 0 ? vo + (t - 3) * 1024 : OOB, 0);
#pragma unroll
            for (int t = 0; t < 2; ++t) vv[t] = LD16N(rv, vo + t * 1024, 0);
#pragma unroll
            for (int t = 0; t < 4; ++t) sw[t] = *(const v4u*)(sk + (64 * xc + 8 * (lane & 7)) * 4 + 8 * t);
            const u32 epoch = LD4S(0, OFF_FL + FL_EPOCH);
            draw = __builtin_amdgcn_raw_buffer_load_b16(mkrs(Dv, 1024), 0, ch * 2, 0);
            const rsrc_t r2 = mkrs(w < 2 ? ssmk + (ch0 + half) * S_ : q, w < 2 ? 4096 : 2 * MB);
            const int qvo = (64 * jb + 32 * half + 4 * (lane >> 3)) * 1024 + xc * 128 + (lane & 7) * 16;
            v4u hq[4], xr[4], wr[8];
            const int wu = 2 * b + half;
            const bool is1 = wu < 176;
            const int u11 = (wu * 5958) >> 16, ua = is1 ? u11 : (wu - 176) >> 2, ub = is1 ? wu - 11 * u11 : wu & 3;
            const int rowb = is1 ? F_ * 2 : D_ * 2;
            const rsrc_t rw = mkrs(is1 ? w1 : w2, wu < 348 && w >= 2 ? F_ * D_ * 2 : 0);
            const int wvo = (32 * ua + (g << (is1 ? 0 : 1))) * rowb + ub * 256 + c * 16;
            Q_LOADS()
            int wdst;
            {
                const int T = 8 * ub + lane, tc = (T * 5958) >> 16, tr = T - 11 * tc, tw = (tr * 22) >> 6, tj = tr - 3 * tw;
                const int o1 = OFF_W1T + (tc * 4 + tw) * 49152 + (((ua - 4 * tw) & 15) * 3 + tj) * 1024;
                const int o2 = OFF_W2T + ((2 * ub + (lane >> 2)) * NP + ua) * 4096 + (lane & 3) * 1024;
                wdst = T < (is1 ? NT : wu < 348 ? 1024 : 0) ? (is1 ? o1 : o2) : OOB;
                PIN_V(wdst);
            }
            SB();
#define W_STORES()                                                                                          \
    {                                                                                                       \
        LAS char* wl = smem + L_Y + half * 16384;                                                           \
        _Pragma("unroll") for (int i = 0; i < 8; ++i) *(LAS v4u*)(wl + i * 1024 + (i >> 1) * 64 + lane * 16) = wr[i]; \
        _Pragma("unroll") for (int blk = 0; blk < 4; ++blk) {                                               \
            u32 d[8];                                                                                       \
            _Pragma("unroll") for (int e = 0; e < 8; ++e) d[e] = *(const LAS u32*)(wl + g * 2112 + c * 4 + e * 256 + blk * 64); \
            _Pragma("unroll") for (int u = 0; u < 2; ++u) {                                                 \
                v4u fr;                                                                                     \
                _Pragma("unroll") for (int e = 0; e < 4; ++e)                                               \
                    fr[e] = __builtin_amdgcn_perm(d[2 * e + 1], d[2 * e], u ? 0x07060302u : 0x05040100u);  \
                ST16S(fr, rws, lane * 16, __builtin_amdgcn_readlane(wdst, 2 * blk + u));                                            \
            }                                                                                               \
        }                                                                                                   \
    }
#pragma unroll
            for (int t = 0; t < 2; ++t) {
                v4u o;
#pragma unroll
                for (int j = 0; j < 4; ++j) {
                    f32x2 a = up2(kk[t][j]) * f32x2{hi2f(sw[j].y), hi2f(sw[j].w)};
                    a = fma2(up2(kk[t + 1][j]), f32x2{lo2f(sw[j].y), lo2f(sw[j].w)}, a);
                    a = fma2(up2(kk[t + 2][j]), f32x2{hi2f(sw[j].x), hi2f(sw[j].z)}, a);
                    a = fma2(up2(kk[t + 3][j]), f32x2{lo2f(sw[j].x), lo2f(sw[j].z)}, a);
                    o[j] = pk2(a * up2(vv[t][j]));
                }
                *(LAS v4u*)(smem + L_P0 + (row + t) * 128 + (lane & 7) * 16) = o;
            }
            tag = mktag(epoch);
            PIN_V(draw);
            PIN_V(pf);
            BAR();
            STAMP(1);
            const int qd = c >> 2;
            if (w < 2) {
                const LAS char* base = smem + L_P0 + 64 * half + 8 * (c & 3) + (63 - 8 * g - qd) * 128;
                const int dst = (64 * xc + 32 * half + c) * 4096 + 16 * g + OFF_UR + (1984 - 64 * jb) * 2;
                v2u a[8];
#pragma unroll
                for (int i = 0; i < 8; ++i) a[i] = tr_read(base + (i >> 2) * 32 - (32 * ((i >> 1) & 1) + 4 * (i & 1)) * 128);
#pragma unroll
                for (int i = 0; i < 4; ++i)
                    ST16S((v4u{a[2 * i].x, a[2 * i].y, a[2 * i + 1].x, a[2 * i + 1].y}), rws, dst, (i & 1) * 64 + (i >> 1) * 65536);
                SB();
                LAS char* cv = smem + half * CV_CH;
                const v4u zero = {0, 0, 0, 0};
                if (lane < 33) *(LAS v4u*)(cv + (lane < 30 ? CV_U + 4096 + lane * 16 : lane < 32 ? CV_E + 16 * (lane - 30) : CV_O)) = zero;
                if (lane == 0) *(LAS v4u*)(cv + CV_O + 16) = v4u{0, 0, 0, hq[0].x << 16};
#pragma unroll
                for (int i = 0; i < 4; ++i) {
                    const u32 nx = i < 3 ? __builtin_amdgcn_readlane(hq[i < 3 ? i + 1 : i].x, 0) : 0u;
                    v4u o;
                    o.x = __builtin_amdgcn_alignbyte(hq[i].y, hq[i].x, 2);
                    o.y = __builtin_amdgcn_alignbyte(hq[i].z, hq[i].y, 2);
                    o.z = __builtin_amdgcn_alignbyte(hq[i].w, hq[i].z, 2);
                    o.w = __builtin_amdgcn_alignbyte((u32)__builtin_amdgcn_update_dpp((int)nx, (int)hq[i].x, 0x130, 0xf, 0xf, false), hq[i].w, 2);
                    *(LAS v4u*)(cv + CV_E + 32 + lane * 16 + i * 1024) = hq[i];
                    *(LAS v4u*)(cv + CV_O + 32 + lane * 16 + i * 1024) = o;
                }
                VMW(0);
#pragma unroll
                for (int i = 0; i < 4; ++i) PIN_V(xr[i]);
                STAMP(2);
                volatile LAS u32* hs = (volatile LAS u32*)(smem + L_ST);
                hs[half] = tag;
                FLAG(hs[half ^ 1] == tag && lane == 0 ? (xc * 32 + jb) * 128 : OOB, OFF_FL + FL_A);
                if (w == 0) {
                    const int fo = OFF_FL + FL_A + xc * 4096, fv = lane < 32 ? lane * 128 : OOB;
                    __builtin_amdgcn_s_sleep(A1DELAY);
                    u32 f = LD4S(fv, fo);
                    if (lane >= 32) f = tag;
                    POLL_FINISH(f, fv, fo);
                    STAMP(5);
                    glds4(ws + OFF_UR + ch0 * 4096, lane * 16, (u32)(size_t)(smem + CV_U));
                    glds4(ws + OFF_UR + ch0 * 4096 + 4096, lane * 16, (u32)(size_t)(smem + CV_CH + CV_U));
                }
            } else {
                LAS char* img = smem + L_P0 + 8192 + half * 4096;
#pragma unroll
                for (int t = 0; t < 4; ++t) *(LAS v4u*)(img + (4 * (lane >> 3) + t) * 128 + (lane & 7) * 16) = hq[t];
                v2u a[8];
#pragma unroll
                for (int i = 0; i < 8; ++i) a[i] = tr_read(img + 8 * (c & 3) + (i >> 1) * 32 + (4 * (i & 1) + 8 * g + qd) * 128);
#pragma unroll
                for (int i = 0; i < 4; ++i)
                    ST16S((v4u{a[2 * i].x, a[2 * i].y, a[2 * i + 1].x, a[2 * i + 1].y}), rws, (64 * xc + c) * 4096 + 16 * g,
                          OFF_X0T + 128 * jb + 64 * half + i * 65536);
                SB();
                f32x4 sta, stb;
#pragma unroll
                for (int i = 0; i < 4; ++i) {
                    f32x2 x[4];
#pragma unroll
                    for (int j = 0; j < 4; ++j) x[j] = up2(xr[i][j]);
                    const f32x2 s2v = (x[0] + x[1]) + (x[2] + x[3]);
                    const f32x2 q2v = fma2(x[0], x[0], x[1] * x[1]) + fma2(x[2], x[2], x[3] * x[3]);
                    sta[i] = s2v.x + s2v.y;
                    stb[i] = q2v.x + q2v.y;
                }
                ROWSUM8(DPP8("1") DPP8("2") DPP8("4") DPP8("8"), sta, stb)
#pragma unroll
                for (int i = 0; i < 4; ++i) {
                    const float mean = xadd32(xadd16(sta[i])) * (1.f / 512.f);
                    const float rs = __builtin_amdgcn_rsqf(fmaxf(xadd32(xadd16(stb[i])) * (1.f / 512.f) - mean * mean, 0.f) + 1e-5f);
                    sta[i] = rs;
                    stb[i] = -mean * rs;
                }
                ST16S(__builtin_bit_cast(v4u, lane ? stb : sta), rws, lane < 2 ? (8 * b + 4 * half) * 4 + lane * 8192 : OOB, OFF_ST0);
                SB();
                W_LOADS()
                VMW(8);
                volatile LAS u32* hs = (volatile LAS u32*)(smem + L_ST + 8);
                hs[half] = tag;
                FLAG(hs[half ^ 1] == tag && lane < 8 ? lane * 1024 + b * 4 : OOB, OFF_FL + FL_A2);
                STAMP(3);
                W_STORES()
                STAMP(4);
            }
        }
        const LAS char* ab[4];
#pragma unroll
        for (int i = 0; i < 4; ++i) {
            ab[i] = smem + cw * CV_CH + ((c & 1) ? CV_E + 2 * (1 + c) : CV_O + 2 * c) + 16 * g + 256 * half + 1024 * i;
            PIN_V(ab[i]);
        }
        const LAS char* bb = smem + cw * CV_CH + CV_U + 4064 - 3584 - 32 * c + 16 * g + 256 * half;
        PIN_V(bb);
        int clr = grp * 65536 + cg * 8192 + w * 1024 + lane * 16 + OFF_X2, clr2 = w == 0 ? grp * 8192 + cg * 1024 + lane * 16 + OFF_ST2 : OOB;
        v4u mark = {~0u, ~0u, ~0u, ~0u};
        asm volatile("" : "+v"(clr), "+v"(clr2), "+v"(mark));
        v4u A[2][8], B[2][8];
#define CV_LAH(buf, r, i, hh) { const LAS u32* pa = (const LAS u32*)(ab[(i) >> 1] + 512 * ((i) & 1) + 64 * (r) + 8 * (hh)); A[buf][i][2 * (hh)] = pa[0]; A[buf][i][2 * (hh) + 1] = pa[1]; }
#define CV_LB(buf, r, i) B[buf][i] = *(const LAS v4u*)(bb + 512 * (7 - (i)) + 64 * (r));
        if (w == 0) VMW(0);
        BAR();
        STAMP(6);
        v4u fa;
        CV_LAH(0, 0, 0, 0) CV_LAH(0, 0, 0, 1)
        SB();
#pragma unroll
        for (int i = 0; i < 8; ++i) { CV_LB(0, 0, i) SB(); }
        CV_LAH(0, 0, 1, 0) CV_LAH(0, 0, 1, 1)
        SB();
        f32x4 acc[8];
        v2u x0[4], uu[4];
        f32x4 sa[4], sb[4];
        const int w1o = OFF_W1T + (cg * 4 + w) * 49152;
        int w1v[3];
#pragma unroll
        for (int j = 0; j < 3; ++j) w1v[j] = T0 + j < Tend ? j * 1024 + lane * 16 : OOB;
        const int tk = 1024 * half + 16 * c + 4 * g;
        const int w2o = OFF_W2T + (cg * NP + w) * 4096, w2v = lane * 16 + 2048 * (w >> 1);
#define W2_LOAD(s, j) W2[s][j] = LD16S(rws, (s) == 10 && w == 3 ? OOB : (w2v ^ (((j) >> 1) * 2048)) + ((j) & 1) * 1024, w2o + (s) * 16384)
#define WSEQ(n)                                                                                                      \
    {                                                                                                                \
        if ((n) < 40) W1[(n) < 40 ? (n) : 0] = LD16S(rws, w1v[((n) + 8) % 3], w1o + (((n) + 8) / 3) * 3072);         \
        else W2_LOAD(((n) < 40 ? 0 : (n) - 40) / 4, ((n) < 40 ? 0 : (n) - 40) % 4);                                  \
    }
        auto conv_step = [&](auto r_) __attribute__((always_inline)) {
            constexpr int r = decltype(r_)::value;
            int idx = 0;
#pragma unroll
            for (int al = 0; al < 8; ++al)
#pragma unroll
                for (int be = 0; be + al < 8; ++be) {
                    MF(r == 0 && al == 0, acc[al + be], "v", A[r & 1][al], "v", B[r & 1][be]);
                    if (r == 0 && idx < 12) CV_LAH(0, 0, 2 + (idx >> 1), idx & 1)
                    {
                        const int t = idx - (r == 0 ? 12 : 4);
                        if (r < 3 && t >= 0 && t < 24) {
                            if (t < 2) CV_LAH((r + 1) & 1, r + 1, 0, t & 1)
                            else if (t < 10) CV_LB((r + 1) & 1, r + 1, t < 10 && t >= 2 ? t - 2 : 0)
                            else CV_LAH((r + 1) & 1, r + 1, t >= 10 ? 1 + ((t - 10) >> 1) : 0, t & 1)
                        }
                    }
                    if (r == 3 && idx < 4) uu[idx] = *(const LAS v2u*)(smem + cw * CV_CH + CV_U + 2 * (2044 - tk) - 512 * idx);
                    if (r == CLRPOS - 1 && idx % 12 == 9)
                        __builtin_amdgcn_raw_buffer_store_b128(mark, rws, idx < 12 ? clr : idx < 24 ? clr + 4096 : clr2, 0, SC1);
                    if (r * 36 + idx == POLI) fa = LD16S(rws, lane * 16, OFF_FL + FL_A2 + xc * 1024);
                    if (r > A2R && ((r - A2R - 1) * 36 + idx) % (((r - A2R - 1) * 36 + idx) < 24 ? 2 : WCD) == 1) {
                        const int t = (r - A2R - 1) * 36 + idx, n = t < 24 ? t / 2 : 12 + (t - 24) / WCD;
                        if (n < 4) x0[n] = LD8S(rws, tk * 2 + n * 512, OFF_X0T + ch * 4096);
                        else if (n < 8) sa[n - 4] = __builtin_bit_cast(f32x4, LD16S(rws, tk * 4 + (n - 4) * 1024, OFF_ST0));
                        else if (n < 12) sb[n - 8] = __builtin_bit_cast(f32x4, LD16S(rws, tk * 4 + (n - 8) * 1024, OFF_ST0 + 8192));
                        else if (n - 12 < Q_CONV) WSEQ(n - 12)
                    }
                    SB();
                    ++idx;
                }
            if (r == PUBR && w >= 2) {
                VMW(PUBR < A2R || PUBR == CLRPOS - 1 ? 0 : 0);
                volatile LAS u32* hs = (volatile LAS u32*)(smem + L_ST + 16);
                hs[half] = tag;
                FLAG(hs[half ^ 1] == tag && lane < 8 ? lane * 1024 + b * 4 : OOB, OFF_FL + FL_W);
            }
            if (r == A2R) {
                POLL_ALL(fa, OFF_FL + FL_A2 + xc * 1024);
                STAMP(7);
            }
        };
        conv_step(IC<0>{});
        conv_step(IC<1>{});
        conv_step(IC<2>{});
        conv_step(IC<3>{});
        MF_DONE();
#pragma unroll
        for (int i = 0; i < 8; ++i) PIN_V(acc[i]);
        STAMP(8);
        LAS f32x4* xch = (LAS f32x4*)(smem + CV_X);
        auto conv_out = [&](auto hb_) __attribute__((always_inline)) {
            constexpr int hb = decltype(hb_)::value;
#pragma unroll
            for (int i = 0; i < 4; ++i) xch[(w * 4 + i) * 64 + lane] = acc[4 - 4 * hb + i];
            SB();
            const float dval = lo2f(draw);
            f32x2 ya[4][2], xa[4][2], na[4][2];
#pragma unroll
            for (int i = 0; i < 4; ++i) {
                ya[i][0] = fma2(f32x2{hi2f(uu[i].y), lo2f(uu[i].y)}, f32x2{dval, dval}, acc[4 * hb + i].xy);
                ya[i][1] = fma2(f32x2{hi2f(uu[i].x), lo2f(uu[i].x)}, f32x2{dval, dval}, acc[4 * hb + i].zw);
                asm volatile("" : "+v"(ya[i][0]), "+v"(ya[i][1]));
            }
            BAR();
            f32x4 oth[4];
#pragma unroll
            for (int i = 0; i < 4; ++i) oth[i] = xch[((w ^ 1) * 4 + i) * 64 + lane];
            SB();
#pragma unroll
            for (int i = 0; i < 4; ++i) {
                xa[i][0] = up2(x0[i].x);
                xa[i][1] = up2(x0[i].y);
                na[i][0] = fma2(xa[i][0], sa[i].xy, sb[i].xy);
                na[i][1] = fma2(xa[i][1], sa[i].zw, sb[i].zw);
            }
            asm volatile("" : "+v"(na[0][0]), "+v"(na[0][1]), "+v"(na[1][0]), "+v"(na[1][1]), "+v"(na[2][0]), "+v"(na[2][1]), "+v"(na[3][0]), "+v"(na[3][1]));
            const int off = (32 * hb + (c >> 1)) * 32768 + ch * 64 + (((c & 1) ^ ((ch >> 2) & 1)) << 5) + 8 * g;
#pragma unroll
            for (int i = 0; i < 4; ++i) {
                ya[i][0] += oth[i].xy;
                ya[i][1] += oth[i].zw;
                ST8S((v2u{pk2(fma2(na[i][0], ya[i][0], xa[i][0])), pk2(fma2(na[i][1], ya[i][1], xa[i][1]))}), rws, off, OFF_X1 + i * 8 * 32768);
            }
            SB();
#pragma unroll
            for (int i = 0; i < 4; ++i) ST8S((v2u{pk2(ya[i][0]), pk2(ya[i][1])}), rws, off, OFF_Y + i * 8 * 32768);
            SB();
        };
        if (half) conv_out(IC<1>{}); else conv_out(IC<0>{});
        STAMP(9);
        const int fb1 = tid < 8 ? tid * 1024 + b * 4 : OOB;
#pragma unroll
        for (int n = Q_CONV; n < Q_Y; ++n) WSEQ(n)
        VMW(Q_Y - Q_CONV);
        BAR();
        FLAG(fb1, OFF_FL + FL_B);
        STAMP(10);
        __builtin_amdgcn_s_sleep(1);
        {
            const v4u fw = LD16S(rws, lane * 16, OFF_FL + FL_W + xc * 1024);
            POLL_ALL(fw, OFF_FL + FL_W + xc * 1024);
        }
        STAMP(13);
        glds8(ws + w1o, lane * 16, (u32)(size_t)smem + L_W + w * 8192);
#pragma unroll
        for (int n = Q_Y; n < Q_B; ++n) WSEQ(n)
        const v4u fb = LD16S(rws, lane * 16, OFF_FL + FL_B + xc * 1024);
        SB();
        {
            v4u ones = {0x3f803f80u, 0x3f803f80u, 0x3f803f80u, 0x3f803f80u};
            PIN_V(ones);
            asm volatile("s_nop 1");
            f32x4 cs[3];
#pragma unroll
            for (int n = 0; n < NCS; ++n) {
                if (n == 0) {
                    VMW(Q_B - Q_Y + 1);
                    v4u wl[8];
#pragma unroll
                    for (int m = 0; m < 8; ++m) wl[m] = *(const LAS v4u*)(smem + L_W + w * 8192 + m * 1024 + lane * 16);
                    SB();
#pragma unroll
                    for (int m = 0; m < 8; ++m) MF(m < 3, cs[m % 3], "v", wl[m], "v", ones);
                }
                if (n < NA1) MF_(cs[(n + 8) % 3], "a", W1[n], "v", ones);
                else MF_(cs[(n + 8) % 3], "v", W1[n], "v", ones);
                SB();
            }
            MF_DONE();
#pragma unroll
            for (int j = 0; j < 3; ++j) {
                PIN_V(cs[j]);
                *(LAS f32x4*)(smem + L_CS + (w * 3 + j) * 64 + g * 16) = cs[j];
            }
        }
        STAMP(11);
        POLL_ALL(fb, OFF_FL + FL_B + xc * 1024);
        STAMP(12);
        const u32 ldsb = (u32)(size_t)smem + w * 8192;
        glds8(ws + OFF_X1 + grp * 65536 + w * 8192, lane * 16, ldsb + L_X);
        FLAG(b == 0 && tid == 0 ? 0 : OOB, OFF_FL + FL_EPOCH);
#pragma unroll
        for (int n = Q_B; n < Q_X; ++n) WSEQ(n)
        VMW(1 + Q_X - Q_B);
    }
    v4u H[HR][2];
    u32 xq[2][2][2];
    v2u yq[2][2];
    f32x4 ps[2];
    auto ffn = [&](auto layer_) __attribute__((always_inline)) {
        constexpr int layer = decltype(layer_)::value;
        int bl = blockIdx.x, w = __builtin_amdgcn_readfirstlane(tid >> 6);
        asm volatile("" : "+s"(bl), "+s"(w));
        const int grp = 4 * (bl & 7) + (bl >> 6), cg = (bl >> 3) & 7;
        const int T0 = 11 * cg + 3 * w, Tend = cg == 7 ? NT : 11 * cg + 11;
        const u32 ldsb = (u32)(size_t)smem + w * 8192 + 4096;
        const char* gb = ws + grp * 65536 + w * 8192 + 4096;
        const rsrc_t rh = mkrs(ws + OFF_H + grp * (2 * NP * 2048), 2 * NP * 2048);
        int lane = tid & 63;
        PIN_V(lane);
        const int c = lane & 15, g = lane >> 4;
        const int w2o = OFF_W2T + (cg * NP + w) * 4096, w2v = lane * 16 + 2048 * (w >> 1);
        const int w1o = OFF_W1T + (cg * 4 + w) * 49152;
        int w1v[3];
#pragma unroll
        for (int j = 0; j < 3; ++j) w1v[j] = T0 + j < Tend ? j * 1024 + lane * 16 : OOB;
        const int hv0 = (w * 2 + (w & 1)) * 1024 + lane * 16, hv1 = hv0 ^ 1024;
        const int fgo = OFF_FL + FL_G + grp * 1024, fvo = (lane & 7) * 128, fsv = tid == 0 ? cg * 128 : OOB;
        const int tt = w & 1, chl = 32 * (w >> 1) + 2 * c;
        const int xo = (64 * cg + chl) * 64 + ((tt ^ ((c >> 1) & 1)) << 5) + 8 * g;
        u32 fl;
#define H_LOAD1(zz, i) H[(zz) % HR][i] = LD16S(rh, (zz) % 11 == 10 && w == 3 ? OOB : ((i) ? hv1 : hv0), (zz) / 11 * NP * 2048 + (zz) % 11 * 8192);
#define W2_LATE(zz)                                                                          \
    if ((zz) < 11 && 4 * (zz) + 44 > Q_H && layer == 0) {                                    \
        _Pragma("unroll") for (int j = 0; j < 4; ++j)                                        \
            if (4 * (zz) + j + 40 >= Q_H) W2_LOAD((zz) < 11 ? (zz) : 0, j);   \
    }
#define H_LOAD(zz) { W2_LATE(zz) H_LOAD1(zz, 0) H_LOAD1(zz, 1) }
#define H_B3(p, q, r) __builtin_amdgcn_bitop3_b32(p, q, r, 0x80)
#define H_BF(zz, p, q) __builtin_amdgcn_bitop3_b32(p, q, (zz) % 11 == 10 && w == 3 ? 0u : 0x80008000u, 0x2a)
#define H_BAD(zz)                                                                              \
    H_BF(zz, H_B3(H[(zz) % HR][0].w, H[(zz) % HR][1].x, H[(zz) % HR][1].y),                    \
         H_B3(H[(zz) % HR][1].z, H[(zz) % HR][1].w, H_B3(H[(zz) % HR][0].x, H[(zz) % HR][0].y, H[(zz) % HR][0].z)))
#define H_RETRY(zz)                                                         \
    {                                                                       \
        u32 spins_ = 0;                                                     \
        while (!dead) {                                                     \
            __builtin_amdgcn_s_sleep(2);                                    \
            asm volatile("" ::: "memory");                                  \
            H_LOAD1(zz, 0) H_LOAD1(zz, 1)                                   \
            if (!__any(H_BAD(zz) != 0u)) break;                             \
            if (++spins_ > SPIN_CAP) dead = true;                           \
        }                                                                   \
    }
#define H_CHECK(zz) if (__builtin_expect(__any(H_BAD(zz) != 0u), 0)) H_RETRY(zz)
#define PS_LOAD(hf)                                                                                         \
    {                                                                                                       \
        gld16<0>(ps[0], ws + OFF_ST2 + (2 * grp + (hf)) * 4096, c * 256 + (16 * tt + 4 * g) * 8);           \
        gld16<16>(ps[1], ws + OFF_ST2 + (2 * grp + (hf)) * 4096, c * 256 + (16 * tt + 4 * g) * 8);          \
    }
#define PS_WAIT(n) asm volatile("s_waitcnt vmcnt(%2)" : "+v"(ps[0]), "+v"(ps[1]) : "n"(n) : "memory");
#define PS_SUM() ROWSUM8(DPP8("1") DPP8("2") DPP8("4") DPP8("8"), ps[0], ps[1])
#define PS_NAN() __any(!((ps[0].y + ps[0].w) + (ps[1].y + ps[1].w) + ((ps[0].x + ps[0].z) + (ps[1].x + ps[1].z)) ==  \
                         (ps[0].y + ps[0].w) + (ps[1].y + ps[1].w) + ((ps[0].x + ps[0].z) + (ps[1].x + ps[1].z))))
#define X3_STAGE(hf, st)                                                                                            \
    {                                                                                                               \
        if ((st) == 0) ROWSUM8(DPP8("1") DPP8("2"), ps[0], ps[1])                                                   \
        if ((st) == 1) ROWSUM8(DPP8("4") DPP8("8"), ps[0], ps[1])                                                   \
        if ((st) == 2) {                                                                                            \
            if (__builtin_expect(PS_NAN(), 0)) {                                                                    \
                u32 spins_ = 0;                                                                                     \
                while (!dead) {                                                                                     \
                    __builtin_amdgcn_s_sleep(2);                                                                    \
                    asm volatile("" ::: "memory");                                                                  \
                    PS_LOAD(hf)                                                                                     \
                    PS_WAIT(0)                                                                                      \
                    PS_SUM()                                                                                        \
                    if (!PS_NAN()) break;                                                                           \
                    if (++spins_ > (SPIN_CAP >> 2)) dead = true;                                                    \
                }                                                                                                   \
            }                                                                                                       \
            _Pragma("unroll") for (int e = 0; e < 2; ++e) {                                                         \
                xb_[e] = f32x2{ps[e].x, ps[e].z} * (1.f / 512.f);                                                   \
                xa_[e] = f32x2{ps[e].y, ps[e].w} * (1.f / 512.f) - xb_[e] * xb_[e];                                 \
            }                                                                                                       \
            asm volatile("" : "+v"(xa_[0]), "+v"(xa_[1]), "+v"(xb_[0]), "+v"(xb_[1]));                              \
        }                                                                                                           \
        if ((st) == 3) {                                                                                            \
            _Pragma("unroll") for (int e = 0; e < 2; ++e) {                                                         \
                xa_[e] = f32x2{__builtin_amdgcn_rsqf(fmaxf(xa_[e].x, 0.f) + 1e-5f), __builtin_amdgcn_rsqf(fmaxf(xa_[e].y, 0.f) + 1e-5f)}; \
                xb_[e] = -xb_[e] * xa_[e];                                                                          \
            }                                                                                                       \
            asm volatile("" : "+v"(xa_[0]), "+v"(xa_[1]), "+v"(xb_[0]), "+v"(xb_[1]));                              \
        }                                                                                                           \
        if ((st) >= 4) {                                                                                            \
            const int j = (st) & 1;                                                                                 \
            const f32x2 x0_ = up2(xq[hf][j][0]), x1_ = up2(xq[hf][j][1]);                                           \
            ST8S((v2u{pk2(fma2(fma2(x0_, xa_[0], xb_[0]), up2(yq[hf][j].x), x0_)),                                  \
                      pk2(fma2(fma2(x1_, xa_[1], xb_[1]), up2(yq[hf][j].y), x1_))}),                                \
                 rws, xo + 64 * j, OFF_X2 + (2 * grp + (hf)) * 32768);                                              \
        }                                                                                                           \
        SB();                                                                                                       \
    }
        f32x2 xa_[2], xb_[2];
#define X_PIECE(hf, n) glds1x(n, gb + (layer ? OFF_X2 : OFF_X1) + (hf) * 32768, lane * 16, ldsb + L_X + (hf) * 32768);
        auto gemm1 = [&](auto hf_) __attribute__((always_inline)) {
            constexpr int hf = decltype(hf_)::value;
            STAMP(16 + layer * 24 + hf * 5);
            const LAS char* xb0[2];
#pragma unroll
            for (int i = 0; i < 2; ++i) xb0[i] = smem + L_X + hf * 32768 + (4 * g + (c >> 2)) * 64 + (((i ^ w ^ g) & 1) << 5) + 8 * (c & 3);
            v4u X[3][2], WL[8];
            f32x4 acc[3][2], accS, accG;
            v4u ones = {0x3f803f80u, 0x3f803f80u, 0x3f803f80u, 0x3f803f80u};
            PIN_V(ones);
#define G1_LOAD(kk, i)                                                                                          \
    {                                                                                                           \
        const LAS char* p_ = xb0[i] + ((((kk) >> 2) + w) & 3) * 8192 + ((kk) & 3) * 2048;                       \
        const v2u lo = tr_read(p_), hi = tr_read(p_ + 1024);                                                    \
        X[(kk) % 3][i] = v4u{lo.x, lo.y, hi.x, hi.y};                                                           \
    }
#define G1_LOADH(kk, i, hh)                                                                                      \
    {                                                                                                           \
        const v2u t_ = tr_read(xb0[i] + ((((kk) >> 2) + w) & 3) * 8192 + ((kk) & 3) * 2048 + (hh) * 1024);      \
        X[(kk) % 3][i][2 * (hh)] = t_.x;                                                                        \
        X[(kk) % 3][i][2 * (hh) + 1] = t_.y;                                                                    \
    }
#define WL_LOAD(n) WL[n] = *(const LAS v4u*)(smem + L_W + w * 8192 + (n) * 1024 + lane * 16);
#define G1_MF(kk, j, i)                                                                                          \
    {                                                                                                            \
        const int n_ = (kk) * 3 + (j) - 8;                                                                       \
        if (n_ < 0) MF((kk) == 0, acc[j][i], "v", WL[n_ < 0 ? n_ + 8 : 0], "v", X[(kk) % 3][i]);                 \
        else if (n_ < NA1) MF_(acc[j][i], "a", W1[n_ < 0 ? 0 : n_], "v", X[(kk) % 3][i]);                        \
        else MF_(acc[j][i], "v", W1[n_ < 0 ? 0 : n_], "v", X[(kk) % 3][i]);                                      \
    }
            WL_LOAD(0) WL_LOAD(1) WL_LOAD(2) WL_LOAD(3) WL_LOAD(4) WL_LOAD(5) WL_LOAD(6) WL_LOAD(7)
            if (hf == 1) VMW(6 + (layer ? 0 : Q_E - Q_A));
            if (hf == 0 && layer == 1) VMW(2);
            STAMP(17 + layer * 24 + hf * 5);
            auto head = [&]() __attribute__((always_inline)) -> bool {
                G1_LOAD(0, 1) G1_LOAD(0, 0) G1_LOAD(1, 1) G1_LOAD(1, 0)
                v4u xs[8];
                u32 mk = 0;
                if (layer == 1) {
#pragma unroll
                    for (int i = 0; i < 8; ++i) xs[i] = *(const LAS v4u*)(smem + L_X + hf * 32768 + w * 8192 + lane * 16 + i * 1024);
                }
                SB();
#pragma unroll
                for (int n = 0; n < 16; ++n) {
                    const int kk = n >> 3, m = n & 7;
                    if (m < 3) G1_MF(kk, m % 3, 0)
                    else if (m == 3) MF(kk == 0, accS, "v", ones, "v", X[kk][0]);
                    else if (m == 4) MF(kk == 0, accG, "v", X[kk][0], "v", X[kk][0]);
                    else G1_MF(kk, (m + 1) % 3, 1)
                    if (m < 4) G1_LOADH(kk + 2, 1 - (m >> 1), m & 1)
                    if (layer == 1) mk = max(max(mk, xs[n >> 1][2 * (n & 1)]), xs[n >> 1][2 * (n & 1) + 1]);
                    if (layer == 1 && (n & 1)) PIN_V(mk);
                    if (layer == 0 && hf == 0 && m == 5) X_PIECE(1, kk)
                    SB();
                }
                return layer == 1 && __any(mk == ~0u);
            };
            if (__builtin_expect(head(), 0)) {
                for (u32 spins = 0; !dead;) {
                    if (++spins > (SPIN_CAP >> 3)) dead = true;
                    asm volatile("s_waitcnt lgkmcnt(0)" ::: "memory");
#pragma unroll
                    for (int n = 0; n < 8; ++n) X_PIECE(hf, n)
                    VMW(0);
                    if (!head()) break;
                }
            }
            STAMP(18 + layer * 24 + hf * 5);
            LAS f32x2* ls = (LAS f32x2*)(smem + L_ST + hf * 512);
            f32x2 s2[2][2];
            f32x4 ct[3];
            f32x2 mv[2], rs2, nb2;
#pragma unroll
            for (int kk = 2; kk < 16; ++kk) {
                if (kk == 2) BAR();
                if (layer == 0 && hf == 1 && kk == FKK) {
                    constexpr int PER = (Q_H - Q_E + WSTEPS - 1) / WSTEPS, NY = Q_E - Q_A + ((FKK - 2) * PER < Q_H - Q_E ? (FKK - 2) * PER : Q_H - Q_E);
                    VMW(NY);
                    BAR();
                    FLAG(fsv, fgo);
                }
                G1_MF(kk, 0, 0)
                if (kk < 14) G1_LOADH(kk + 2, 1, 0)
                if (kk == 14) { rs2 = f32x2{__builtin_amdgcn_rsqf(rs2.x), __builtin_amdgcn_rsqf(rs2.y)}; PIN_V(rs2); }
                SB();
                G1_MF(kk, 1, 0)
                if (kk < 14) G1_LOADH(kk + 2, 1, 1)
                if (kk == 14) { nb2 = f32x2{mv[0].x, mv[1].x} * (layer ? rs2 : -rs2); if (layer) rs2 = -rs2; asm volatile("" : "+v"(rs2), "+v"(nb2)); }
                SB();
                G1_MF(kk, 2, 0)
                if (kk < 14) G1_LOADH(kk + 2, 0, 0)
                SB();
                if (kk < 8) {
                    MF_(accS, "v", ones, "v", X[kk % 3][0]);
                    MF_(accG, "v", X[kk % 3][0], "v", X[kk % 3][0]);
                }
                G1_MF(kk, 0, 1)
                if (kk < 14) G1_LOADH(kk + 2, 0, 1)
                SB();
                G1_MF(kk, 1, 1)
                if (layer == 0 && hf == 0 && kk < 8) X_PIECE(1, kk)
                if (layer == 1 && hf == 0 && kk >= XKK && kk < XKK + 4) X_PIECE(1, 2 * (kk - XKK))
                if (layer == 0 && hf == 1 && kk == CPOLL) fl = LD4S(fvo, fgo);
                if (layer == 1 && hf == 1 && kk >= 16 - HR) H_LOAD1(kk - (16 - HR), 0)
                if (kk == 8) {
                    asm volatile("" : "+v"(accS), "+v"(accG));
                    const float g0 = accG.x, g1 = accG.y, g2 = accG.z, g3 = accG.w;
                    mv[0] = f32x2{c & 1 ? g1 : g0, c & 1 ? g3 : g2};
                    PIN_V(mv[0]);
                }
                if (kk == 9) ls[g == (c >> 2) ? w * 16 + c : 128 + lane] = f32x2{accS.x, c & 2 ? mv[0].y : mv[0].x};
                if (kk == 11) {
#pragma unroll
                    for (int i = 0; i < 2; ++i) {
                        s2[i][0] = ls[((i ^ w) & 1) * 16 + c];
                        s2[i][1] = ls[(((i ^ w) & 1) + 2) * 16 + c];
                    }
                }
                if (kk == 12) {
#pragma unroll
                    for (int j = 0; j < 3; ++j) ct[j] = *(const LAS f32x4*)(smem + L_CS + (w * 3 + j) * 64 + g * 16);
                }
                if (kk == 13) {
#pragma unroll
                    for (int i = 0; i < 2; ++i) mv[i] = (s2[i][0] + s2[i][1]) * (1.f / 512.f);
                    asm volatile("" : "+v"(mv[0]), "+v"(mv[1]));
                }
                SB();
                G1_MF(kk, 2, 1)
                if (layer == 1 && hf == 0 && kk >= XKK && kk < XKK + 4) X_PIECE(1, 2 * (kk - XKK) + 1)
                if (layer == 1 && hf == 1 && kk >= 16 - HR) H_LOAD1(kk - (16 - HR), 1)
                if (kk == 13) {
                    rs2 = f32x2{fmaxf(mv[0].y - mv[0].x * mv[0].x, 0.f) + 1e-5f, fmaxf(mv[1].y - mv[1].x * mv[1].x, 0.f) + 1e-5f};
                    PIN_V(rs2);
                }
                if (layer == 0) {
                    constexpr int N0 = hf ? Q_E : Q_X, N1 = hf ? Q_H : Q_A, PER = (N1 - N0 + WSTEPS - 1) / WSTEPS;
#pragma unroll
                    for (int e = 0; e < 6; ++e) {
                        const int n = N0 + (kk - 2) * PER + e;
                        if (e < PER && n < N1) WSEQ(n)
                    }
                }
                if (kk == 10) BAR();
                SB();
            }
            const float rs[2] = {rs2.x, rs2.y}, nb[2] = {nb2.x, nb2.y};
            STAMP(19 + layer * 24 + hf * 5);
            if (hf == 0 && NCS < 40 && layer == 0) {
#pragma unroll
                for (int n = NCS; n < 40; ++n) {
                    if (n < NA1) MF_(ct[(n + 8) % 3], "a", W1[n], "v", ones);
                    else MF_(ct[(n + 8) % 3], "v", W1[n], "v", ones);
                }
                MF_DONE();
#pragma unroll
                for (int j = 0; j < 3; ++j) {
                    PIN_V(ct[j]);
                    *(LAS f32x4*)(smem + L_CS + (w * 3 + j) * 64 + g * 16) = ct[j];
                }
            }
            if (layer == 0 && hf == 1) {
                POLL_FINISH(fl, fvo, fgo);
#pragma unroll
                for (int z = 0; z < NPRE; ++z) H_LOAD(z)
                SB();
            }
#pragma unroll
            for (int i = 0; i < 2; ++i) {
                f32x4 u[3];
#pragma unroll
                for (int j = 0; j < 3; ++j) u[j] = ct[j] * nb[i];
                asm volatile("" : "+v"(u[0]), "+v"(u[1]), "+v"(u[2]));
                if (i == 0) MF_DONE();
                asm volatile("" : "+v"(acc[0][i]), "+v"(acc[1][i]), "+v"(acc[2][i]));
#pragma unroll
                for (int j = 0; j < 3; ++j) u[j] = acc[j][i] * rs[i] + u[j];
                v2u o[3];
#pragma unroll
                for (int j = 0; j < 3; ++j) o[j] = v2u{pk2(u[j].xy), pk2(u[j].zw)};
                asm volatile("" : "+v"(o[0]), "+v"(o[1]), "+v"(o[2]));
#pragma unroll
                for (int j = 0; j < 3; ++j) {
                    const int T = T0 + j;
                    ST8S((v2u{relu2<layer>(o[j].x), relu2<layer>(o[j].y)}), rh,
                         T < Tend ? (((i ^ w) & 1) * 1024 + (g >> 1) * 256 + c * 16 + (g & 1) * 8) : OOB,
                         hf * NP * 2048 + (T >> 1) * 2048 + 512 * (T & 1));
                }
                SB();
            }
            if (layer == 0 && hf == 0) {
#pragma unroll
                for (int n = Q_A; n < Q_E; ++n) WSEQ(n)
                SB();
            }
            if (layer == 0 && hf == 1) {
#pragma unroll
                for (int z = NPRE; z < HR; ++z) H_LOAD(z)
                SB();
            }
            STAMP(20 + layer * 24 + hf * 5);
        };
        gemm1(IC<0>{});
        gemm1(IC<1>{});
        if (layer == 1) { H_CHECK(0) }
        SB();
        STAMP(26 + layer * 24);
        auto gemm2 = [&](auto hf_) __attribute__((always_inline)) {
            constexpr int hf = decltype(hf_)::value;
            f32x4 acc[2][4];
            v2u res[2];
            LAS f32x4* red = (LAS f32x4*)(smem + L_RED);
#define RED_PUT(n) { PIN_V(acc[(n) >> 2][(n) & 3]); red[(((w ^ (((n) >> 2) + 2 * (((n) & 3) >> 1))) * 3 + ((n) >> 2) + 2 * (((n) & 3) >> 1) - 1) * 2 + ((n) & 1)) * 64 + lane] = acc[(n) >> 2][(n) & 3]; SB(); }
#pragma unroll
            for (int s = 0; s < 11; ++s) {
                const int z = 11 * hf + s;
                const bool chk = layer == 1 && s < 10, more = layer == 1 ? z + HR < 22 : s + HR < 11;
#define G2_MF(i, j) MF(s == 0, acc[i][j], "v", H[z % HR][i], "a", W2[s][j]);
                u32 b0 = 0, b1 = 0;
                G2_MF(1, 3)
                G2_MF(1, 2)
                if (chk) { b0 = H_B3(H[(z + 1) % HR][0].x, H[(z + 1) % HR][0].y, H[(z + 1) % HR][0].z); PIN_V(b0); }
                G2_MF(1, 1)
                if (chk) {
                    b1 = H_B3(H[(z + 1) % HR][0].w, H[(z + 1) % HR][1].x, H[(z + 1) % HR][1].y);
                    b0 = H_B3(H[(z + 1) % HR][1].z, H[(z + 1) % HR][1].w, b0);
                    asm volatile("" : "+v"(b0), "+v"(b1));
                }
                G2_MF(1, 0)
                bool bad = false;
                if (chk) bad = __any(H_BF(z + 1, b0, b1) != 0u);
                SB();
                G2_MF(0, 3)
                if (more) { W2_LATE(z + HR) H_LOAD1(z + HR, 1) }
                SB();
                G2_MF(0, 2)
                SB();
                if (chk) { if (__builtin_expect(bad, 0)) H_RETRY(z + 1) }
                if (s == 10) { asm volatile("s_nop 2" ::: "memory"); RED_PUT(7) }
                G2_MF(0, 1)
                if (s == 10) RED_PUT(6)
                G2_MF(0, 0)
                SB();
                if (s == (layer == 0 && hf == 0 ? NPRE : 4) && (hf == 1 || layer == 0)) {
                    BAR();
                    if (hf == 0) FLAG(fsv, fgo + 32768);
                }
                if (more) H_LOAD1(z + HR, 0)
                if (layer == 0 && hf == 0 && s == CPOLL2) fl = LD4S(fvo, fgo + 32768);
                if (layer == 0 && s == 5) {
#pragma unroll
                    for (int j = 0; j < 2; ++j) yq[hf][j] = LD8S(rws, xo + 64 * j, OFF_Y + (2 * grp + hf) * 32768);
                }
                if (layer == 0 && hf == 1 && s == SQ) {
#pragma unroll
                    for (int zz = z + 1; zz < 22; ++zz) asm volatile("" : "+v"(H[zz % HR][0]), "+v"(H[zz % HR][1]));
                    PS_LOAD(0)
                }
                if (s == 8) {
                    res[0] = *(const LAS v2u*)(smem + L_X + hf * 32768 + xo);
                    res[1] = *(const LAS v2u*)(smem + L_X + hf * 32768 + xo + 64);
                }
                SB();
            }
            STAMP(27 + layer * 24 + hf * 4);
            RED_PUT(5) RED_PUT(4) RED_PUT(3) RED_PUT(2)
            f32x2 xf[2][2];
            asm volatile("s_nop 1" : "+v"(acc[0][0]), "+v"(acc[0][1]));
#pragma unroll
            for (int j = 0; j < 2; ++j) {
                xf[j][0] = layer ? up2(res[j].x) - acc[0][j].xy : up2(res[j].x) + acc[0][j].xy;
                xf[j][1] = layer ? up2(res[j].y) - acc[0][j].zw : up2(res[j].y) + acc[0][j].zw;
            }
            BAR();
            f32x4 rd[6];
#pragma unroll
            for (int i = 0; i < 6; ++i) rd[i] = red[(w * 6 + i) * 64 + lane];
            SB();
            STAMP(28 + layer * 24 + hf * 4);
            if (layer == 0 && hf == 0) {
                POLL_FINISH(fl, fvo, fgo + 32768);
#pragma unroll
                for (int z = 11; z < 11 + NPRE2; ++z) H_LOAD(z)
                SB();
            }
#pragma unroll
            for (int j = 0; j < 2; ++j) {
                const f32x4 t = rd[j] + (rd[2 + j] + rd[4 + j]);
                xf[j][0] = layer ? xf[j][0] - t.xy : xf[j][0] + t.xy;
                xf[j][1] = layer ? xf[j][1] - t.zw : xf[j][1] + t.zw;
            }
            STAMP(29 + layer * 24 + hf * 4);
            if (layer == 1) {
#pragma unroll
                for (int r = 0; r < 4; ++r)
                    __builtin_amdgcn_raw_buffer_store_b32(pk2bf(xf[0][r >> 1][r & 1], xf[1][r >> 1][r & 1]), mkrs(out, 2 * MB),
                                                          (16 * tt + 4 * g + r) * 1024 + chl * 2, (64 * grp + 32 * hf) * 1024 + cg * 128, 0);
            } else {
#pragma unroll
                for (int j = 0; j < 2; ++j) {
                    xq[hf][j][0] = pk2(xf[j][0]);
                    xq[hf][j][1] = pk2(xf[j][1]);
                }
                f32x2 sm[2], sq[2];
#pragma unroll
                for (int r = 0; r < 2; ++r) {
                    const f32x2 x0 = up2(xq[hf][0][r]), x1 = up2(xq[hf][1][r]);
                    sm[r] = x0 + x1;
                    sq[r] = fma2(x0, x0, x1 * x1);
                }
                f32x4 s4;
                {
                    const bool odd = c & 1;
                    const f32x4 keep = odd ? f32x4{sm[1].x, sq[1].x, sm[1].y, sq[1].y} : f32x4{sm[0].x, sq[0].x, sm[0].y, sq[0].y};
                    const f32x4 give = odd ? f32x4{sm[0].x, sq[0].x, sm[0].y, sq[0].y} : f32x4{sm[1].x, sq[1].x, sm[1].y, sq[1].y};
                    float e0 = keep.x, e1 = keep.y, e2 = keep.z, e3 = keep.w;
                    const float g0 = give.x, g1 = give.y, g2 = give.z, g3 = give.w;
                    asm volatile("s_nop 1\n\t"
                                 "v_add_f32_dpp %0, %4, %0 quad_perm:[1,0,3,2] row_mask:0xf bank_mask:0xf\n\t"
                                 "v_add_f32_dpp %1, %5, %1 quad_perm:[1,0,3,2] row_mask:0xf bank_mask:0xf\n\t"
                                 "v_add_f32_dpp %2, %6, %2 quad_perm:[1,0,3,2] row_mask:0xf bank_mask:0xf\n\t"
                                 "v_add_f32_dpp %3, %7, %3 quad_perm:[1,0,3,2] row_mask:0xf bank_mask:0xf\n\t"
                                 DPP4("2") DPP4("4") DPP4("8")
                                 : "+v"(e0), "+v"(e1), "+v"(e2), "+v"(e3) : "v"(g0), "v"(g1), "v"(g2), "v"(g3));
                    s4 = f32x4{e0, e1, e2, e3};
                }
                ST16S(__builtin_bit_cast(v4u, s4), rws, c < 2 ? (cg * 2 + (w >> 1)) * 256 + (16 * tt + 4 * g + 2 * c) * 8 : OOB,
                      OFF_ST2 + (2 * grp + hf) * 4096);
            }
            SB();
            if (hf == 0 && layer == 0) {
#pragma unroll
                for (int z = 11 + NPRE2; z < 11 + HR; ++z) H_LOAD(z)
                SB();
            }
            if (hf == 0 && layer == 1) { H_CHECK(11) }
            if (hf == 1 && layer == 0) {
#define X3_PUBLISH(hf) X3_STAGE(hf, 0) X3_STAGE(hf, 1) X3_STAGE(hf, 2) X3_STAGE(hf, 3) X3_STAGE(hf, 4) X3_STAGE(hf, 5)
                PS_WAIT(1)
                X3_PUBLISH(0)
                STAMP(35);
                __builtin_amdgcn_s_sleep(PSLEEP2);
                asm volatile("" ::: "memory");
                PS_LOAD(1)
                SB();
                __builtin_amdgcn_s_sleep(XSLEEP);
                glds8(gb - 4096 + OFF_X2, lane * 16, ldsb - 4096 + L_X);
                STAMP(36);
                PS_WAIT(8)
                X3_PUBLISH(1)
            }
            STAMP(30 + layer * 24 + hf * 4);
        };
        gemm2(IC<0>{});
        gemm2(IC<1>{});
    };
    ffn(IC<0>{});
    ffn(IC<1>{});
    asm volatile(".Lpf_end:");
}

extern "C" void kernel_launch(void* const* d_in, const int* in_sizes, int n_in, void* d_out, int out_size, void* d_ws,
                              size_t ws_size, hipStream_t stream) {
    constexpr int lds = L_BYTES;
    (void)hipFuncSetAttribute((const void*)k_all, hipFuncAttributeMaxDynamicSharedMemorySize, lds);
    k_all<<<256, 256, lds, stream>>>((const u16*)d_in[0], (const u16*)d_in[1], (const u16*)d_in[2], (const u16*)d_in[3],
                                     (const u16*)d_in[4], (const u16*)d_in[5], (const u16*)d_in[7], (const u16*)d_in[8],
                                     (u16*)d_out, (char*)d_ws + WSOFF);
}
```
